```python
import torch

def _softmax(x, axis=-1):
    return torch.softmax(x, dim=axis)
def _gelu(x):
    return torch.nn.functional.gelu(x, approximate="tanh")
def _silu(x):
    return torch.nn.functional.silu(x)
def _sigmoid(x):
    return torch.sigmoid(x)
def _layernorm(x, axis=-1, eps=1e-5):
    m = x.mean(dim=axis, keepdim=True)
    v = x.var(dim=axis, keepdim=True, unbiased=False)
    return (x - m) / torch.sqrt(v + eps)
def _scatter_add(tgt, idx, src, axis=0):
    return tgt.clone().index_add_(axis, idx, src)
def _segment_sum(src, seg, n):
    out = torch.zeros((n,) + src.shape[1:], dtype=src.dtype)
    return out.index_add_(0, seg, src)
def _pad(x, pw):
    flat = tuple(v for pair in reversed(pw) for v in pair)
    return torch.nn.functional.pad(x, flat)

kernel_name = 'standalone__gqa_softmax__near_ridge'

def setup_inputs():

    _w = (lambda a: (lambda t: t.to(torch.bfloat16) if t.is_floating_point() else t)(torch.as_tensor(a)))
    B, Hq, Hkv, Sq, Sk, D = 32, 32, 32, 256, 512, 128
    return {
        "q": _w((lambda *s: torch.randn(*s, dtype=torch.bfloat16) * 0.8)(B, Hq, Sq, D)),
        "k": _w((lambda *s: torch.randn(*s, dtype=torch.bfloat16) * 0.8)(B, Hkv, Sk, D)),
        "v": _w((lambda *s: torch.randn(*s, dtype=torch.bfloat16) * 0.05)(B, Hkv, Sk, D)),
    }

def reference(q, k, v):
    B, Hq, Sq, D = q.shape
    Hkv = k.shape[1]
    rep = Hq // Hkv
    k = (lambda x, n, axis: torch.repeat_interleave(x, n, dim=axis))(k, rep, axis=1)
    v = (lambda x, n, axis: torch.repeat_interleave(x, n, dim=axis))(v, rep, axis=1)
    scale = D ** -0.5
    scores = torch.einsum("bhqd,bhkd->bhqk", q, k) * scale
    p = _softmax(scores, axis=-1)
    return torch.einsum("bhqk,bhkd->bhqd", p, v)
```

```cpp
#include <hip/hip_runtime.h>
#include <hip/hip_bf16.h>
#include <cstdio>

using bf16 = __hip_bfloat16;
constexpr int   D = 128, NW = 8, QBLK = 32, KVBLK = 64;
constexpr float SCALE = 0.088388347648318440f;
#define ATTN_THR 8.f
constexpr float THR = ATTN_THR;
#define ATTN_LAYOUT 0
constexpr int LAYOUT = ATTN_LAYOUT;
#define ATTN_SDEPTH 2
constexpr int SDEPTH = ATTN_SDEPTH;
static_assert(SDEPTH == 1 || SDEPTH == 2, "SDEPTH is 1 or 2");
#define ATTN_B 32
#define ATTN_HQ 32
#define ATTN_HKV 32
#define ATTN_S 256
#define ATTN_SKV 512
constexpr int B = ATTN_B, HQ = ATTN_HQ, HKV = ATTN_HKV, S = ATTN_S, SKV = ATTN_SKV;
constexpr int LDQ = LAYOUT ? HQ * D : D, LDK = LAYOUT ? HKV * D : D, LDO = LDQ;
static_assert(S % (QBLK * NW) == 0 && SKV % (2 * KVBLK) == 0 && HQ % HKV == 0, "S % 256 == 0, SKV % 128 == 0 (even tile count), HQ % HKV == 0");
constexpr size_t SHM_V = KVBLK * D * 2, SHM_K = KVBLK * D * 2, SHM_ATTN = 2 * SHM_V + 2 * SHM_K + NW * 64 * 4;

using bf16x8 = __attribute__((ext_vector_type(8))) short;
using s16x4  = __attribute__((ext_vector_type(4))) short;
using f32x16 = __attribute__((ext_vector_type(16))) float;
using f32x8  = __attribute__((ext_vector_type(8))) float;
using u32x4  = __attribute__((ext_vector_type(4))) unsigned;
#define KSWZ(row, colB) ((row) * 256 + ((colB) ^ (((row) & 7) << 4)))
#define SBAR() __builtin_amdgcn_sched_barrier(0)
__device__ __forceinline__ int crow(int r, int hi) { return (r & 3) + 8 * (r >> 2) + 4 * hi; }
__device__ __forceinline__ unsigned cvtpk(float lo, float hi) {
  unsigned r; asm volatile("v_cvt_pk_bf16_f32 %0, %1, %2" : "=v"(r) : "v"(lo), "v"(hi)); return r;
}
template <typename TIn> struct Stage;
template <> struct Stage<bf16>  { using T = bf16x8;
  __device__ static __forceinline__ T ld8(const bf16* p) { return *reinterpret_cast<const bf16x8*>(p); }
  __device__ static __forceinline__ bf16x8 tobf(T x) { return x; } };
template <> struct Stage<float> { using T = f32x8;
  __device__ static __forceinline__ T ld8(const float* p) { return *reinterpret_cast<const f32x8*>(p); }
  __device__ static __forceinline__ bf16x8 tobf(T x) {
    u32x4 w = {cvtpk(x[0], x[1]), cvtpk(x[2], x[3]), cvtpk(x[4], x[5]), cvtpk(x[6], x[7])}; return *reinterpret_cast<bf16x8*>(&w); } };

__device__ __forceinline__ void partialSM(f32x16& p0, f32x16& p1, float& m_reg, float& mn, float& alpha) {
  constexpr float C = SCALE * 1.4426950408889634f;
  float pmax = p0[0]; for (int r = 1; r < 16; ++r) pmax = fmaxf(pmax, p0[r]); for (int r = 0; r < 16; ++r) pmax = fmaxf(pmax, p1[r]);
  { auto rr = __builtin_amdgcn_permlane32_swap(__float_as_uint(pmax), __float_as_uint(pmax), false, false);
    pmax = fmaxf(__uint_as_float(rr[0]), __uint_as_float(rr[1])); }
  if (__builtin_expect(__all(pmax - m_reg <= THR / SCALE), 1)) { mn = m_reg; alpha = 1.f; }
  else { mn = fmaxf(m_reg, pmax); alpha = __builtin_amdgcn_exp2f((m_reg - mn) * C); m_reg = mn; }
  float mnC = -mn * C;
  for (int r = 0; r < 16; ++r) p0[r] = fmaf(p0[r], C, mnC); for (int r = 0; r < 16; ++r) p1[r] = fmaf(p1[r], C, mnC);
  for (int r = 0; r < 16; ++r) p0[r] = __builtin_amdgcn_exp2f(p0[r]);
}
__device__ __forceinline__ void finishSM(f32x16& p0, f32x16& p1, float alpha, float& l_reg, bf16x8& pa0, bf16x8& pa1, bf16x8& pa2, bf16x8& pa3) {
  for (int r = 0; r < 16; ++r) p1[r] = __builtin_amdgcn_exp2f(p1[r]);
  float ps = 0; for (int r = 0; r < 16; ++r) ps += p0[r]; for (int r = 0; r < 16; ++r) ps += p1[r];
  { auto rr = __builtin_amdgcn_permlane32_swap(__float_as_uint(ps), __float_as_uint(ps), false, false);
    ps = __uint_as_float(rr[0]) + __uint_as_float(rr[1]); }
  l_reg = l_reg * alpha + ps;
#define PK4(P, BASE, OUT) do { unsigned a0 = cvtpk(P[BASE + 0], P[BASE + 1]), a1 = cvtpk(P[BASE + 2], P[BASE + 3]);   \
    unsigned b0 = cvtpk(P[BASE + 4], P[BASE + 5]), b1 = cvtpk(P[BASE + 6], P[BASE + 7]);                              \
    auto r0 = __builtin_amdgcn_permlane32_swap(a0, b0, false, false); auto r1 = __builtin_amdgcn_permlane32_swap(a1, b1, false, false); \
    u32x4 w = {r0[0], r1[0], r0[1], r1[1]}; OUT = *reinterpret_cast<bf16x8*>(&w); } while (0)
  PK4(p0, 0, pa0); PK4(p0, 8, pa1); PK4(p1, 0, pa2); PK4(p1, 8, pa3);
#undef PK4
}
__device__ __forceinline__ void qkt(f32x16& p0, f32x16& p1, const bf16* Ks, const bf16x8* qr, int r32, int hi) {
  p0 = f32x16{}; p1 = f32x16{};
  for (int d0 = 0; d0 < 8; ++d0) { int cb = (d0 * 16 + hi * 8) * 2;
    bf16x8 b0 = *reinterpret_cast<const bf16x8*>((const char*)Ks + KSWZ(r32, cb));
    bf16x8 b1 = *reinterpret_cast<const bf16x8*>((const char*)Ks + KSWZ(32 + r32, cb));
    p0 = __builtin_amdgcn_mfma_f32_32x32x16_bf16(b0, qr[d0], p0, 0, 0, 0);
    p1 = __builtin_amdgcn_mfma_f32_32x32x16_bf16(b1, qr[d0], p1, 0, 0, 0); }
}
__device__ __forceinline__ int v_st(int k, int c) { const int kk = (k & ~0xC) | ((k & 4) << 1) | ((k & 8) >> 1); return ((kk >> 3) * 4 + (c >> 5)) * 512 + ((kk & 7) * 32 + (c & 31)) * 2; }
__device__ __forceinline__ int v_rd_base(int lane) { return ((lane & 3) << 3) | (((lane >> 2) & 3) << 6) | (((lane >> 4) & 1) << 5) | (((lane >> 5) & 1) << 8); }
constexpr int v_rd_off(int d0, int ks, int half) { return d0 * 512 + ks * 4096 + half * 2048; }
template <int OFF> __device__ __forceinline__ s16x4 tr_read(int vb) {
  s16x4 r; asm volatile("ds_read_b64_tr_b16 %0, %1 offset:%2" : "=&v"(r) : "v"(vb), "i"(OFF) : "memory"); return r;
}
template <int D0> __device__ __forceinline__ void pv_one(f32x16& od, int vb, bf16x8 pa0, bf16x8 pa1, bf16x8 pa2, bf16x8 pa3) {
  const s16x4 l0 = tr_read<v_rd_off(D0, 0, 0)>(vb), h0 = tr_read<v_rd_off(D0, 0, 1)>(vb), l1 = tr_read<v_rd_off(D0, 1, 0)>(vb), h1 = tr_read<v_rd_off(D0, 1, 1)>(vb);
  const s16x4 l2 = tr_read<v_rd_off(D0, 2, 0)>(vb), h2 = tr_read<v_rd_off(D0, 2, 1)>(vb), l3 = tr_read<v_rd_off(D0, 3, 0)>(vb), h3 = tr_read<v_rd_off(D0, 3, 1)>(vb);
  asm volatile("s_waitcnt lgkmcnt(0)" ::: "memory"); SBAR();
#define PK(L, H) (bf16x8){L[0], L[1], L[2], L[3], H[0], H[1], H[2], H[3]}
  od = __builtin_amdgcn_mfma_f32_32x32x16_bf16(pa0, PK(l0, h0), od, 0, 0, 0);
  od = __builtin_amdgcn_mfma_f32_32x32x16_bf16(pa1, PK(l1, h1), od, 0, 0, 0);
  od = __builtin_amdgcn_mfma_f32_32x32x16_bf16(pa2, PK(l2, h2), od, 0, 0, 0);
  od = __builtin_amdgcn_mfma_f32_32x32x16_bf16(pa3, PK(l3, h3), od, 0, 0, 0);
#undef PK
}
__device__ __forceinline__ void pv_d0(f32x16* o, int vb, bf16x8 pa0, bf16x8 pa1, bf16x8 pa2, bf16x8 pa3) {
  pv_one<0>(o[0], vb, pa0, pa1, pa2, pa3); pv_one<1>(o[1], vb, pa0, pa1, pa2, pa3); pv_one<2>(o[2], vb, pa0, pa1, pa2, pa3); pv_one<3>(o[3], vb, pa0, pa1, pa2, pa3);
}

template <typename TQ>
__device__ __forceinline__ void attn_dense_body(const TQ* __restrict__ Qb, const bf16* __restrict__ Kh, const bf16* __restrict__ Vh,
                                                bf16* __restrict__ Ob, int seq, char* lds) {
  using St = Stage<bf16>; using SQ = Stage<TQ>;
  const int tid = threadIdx.x, wid = tid >> 6, lane = tid & 63, r32 = lane & 31, hi = lane >> 5;
  bf16* V_lds = (bf16*)lds; bf16* K_lds = (bf16*)(lds + 2 * SHM_V);
  float* ws = (float*)(lds + 2 * SHM_V + 2 * SHM_K) + wid * 64; float* li_l = ws; float* al_l = ws + 32;
  float m_reg = -1e30f, l_reg = 0; f32x16 o[4] = {}; bf16x8 qr[8];
  const TQ* Qw = Qb + (long)(wid * QBLK + r32) * LDQ + hi * 8;
#pragma unroll
  for (int d0 = 0; d0 < 8; ++d0) qr[d0] = SQ::tobf(SQ::ld8(Qw + d0 * 16));
  const int sr = tid >> 4, sc = (tid & 15) * 8, vst0 = v_st(sr, sc), vst1 = v_st(32 + sr, sc);
  const int vb0 = (int)(uintptr_t)V_lds + v_rd_base(lane);
  struct { typename St::T vs0, vs1, ks0, ks1; } sr_[SDEPTH];
#define SLOAD(i, k0) do { sr_[i].vs0 = St::ld8(&Vh[(long)((k0) + sr) * LDK + sc]); sr_[i].vs1 = St::ld8(&Vh[(long)((k0) + 32 + sr) * LDK + sc]); \
    sr_[i].ks0 = St::ld8(&Kh[(long)((k0) + sr) * LDK + sc]); sr_[i].ks1 = St::ld8(&Kh[(long)((k0) + 32 + sr) * LDK + sc]); } while (0)
#define SWRITE(b, i) do { *(bf16x8*)((char*)V_lds + (b) * SHM_V + vst0) = St::tobf(sr_[i].vs0);          \
    *(bf16x8*)((char*)V_lds + (b) * SHM_V + vst1) = St::tobf(sr_[i].vs1); int kc = sc * 2;               \
    *(bf16x8*)((char*)K_lds + (b) * SHM_K + KSWZ(sr, kc)) = St::tobf(sr_[i].ks0);                       \
    *(bf16x8*)((char*)K_lds + (b) * SHM_K + KSWZ(32 + sr, kc)) = St::tobf(sr_[i].ks1); } while (0)
#define SWAIT() do { if constexpr (SDEPTH == 2) asm volatile("s_waitcnt vmcnt(4)" ::: "memory"); else asm volatile("s_waitcnt vmcnt(0)" ::: "memory"); } while (0)
#define RESC(a) do { if (__any((a) < 1.f)) { if (hi == 0) al_l[r32] = (a); asm volatile("s_waitcnt lgkmcnt(0)" ::: "memory"); \
    for (int d = 0; d < 4; ++d) for (int r = 0; r < 16; ++r) o[d][r] *= al_l[crow(r, hi)]; } } while (0)
  f32x16 pA0, pA1, pB0, pB1; float mnA, mnB, alA, alB; bf16x8 pa0, pa1, pa2, pa3; const int NT = seq / KVBLK;
  constexpr int SE = 0, SO = SDEPTH - 1;
  SLOAD(SE, 0); asm volatile("s_waitcnt vmcnt(0)" ::: "memory"); SWRITE(0, SE); __syncthreads();
  qkt(pA0, pA1, K_lds, qr, r32, hi); partialSM(pA0, pA1, m_reg, mnA, alA);
  SLOAD(SO, KVBLK); if constexpr (SDEPTH == 2) { if (2 < NT) SLOAD(SE, 2 * KVBLK); }
  SWAIT(); SWRITE(1, SO); __syncthreads();
  for (int j = 1; j + 1 < NT; j += 2) {
    SBAR(); qkt(pB0, pB1, (bf16*)((char*)K_lds + SHM_K), qr, r32, hi);
    finishSM(pA0, pA1, alA, l_reg, pa0, pa1, pa2, pa3); SBAR();
    SLOAD(SO, (j + SDEPTH) * KVBLK); SBAR();
    pv_d0(o, vb0, pa0, pa1, pa2, pa3); partialSM(pB0, pB1, m_reg, mnB, alB);
    __syncthreads(); SWAIT(); SWRITE(0, SE);
    RESC(alB); __syncthreads();
    SBAR(); qkt(pA0, pA1, K_lds, qr, r32, hi);
    finishSM(pB0, pB1, alB, l_reg, pa0, pa1, pa2, pa3); SBAR();
    if (SDEPTH == 1 || j + 3 < NT) SLOAD(SE, (j + 1 + SDEPTH) * KVBLK); SBAR();
    pv_d0(o, vb0 + (int)SHM_V, pa0, pa1, pa2, pa3); partialSM(pA0, pA1, m_reg, mnA, alA);
    __syncthreads(); SWAIT(); SWRITE(1, SO);
    RESC(alA); __syncthreads();
  }
  SBAR(); qkt(pB0, pB1, (bf16*)((char*)K_lds + SHM_K), qr, r32, hi);
  finishSM(pA0, pA1, alA, l_reg, pa0, pa1, pa2, pa3); SBAR();
  pv_d0(o, vb0, pa0, pa1, pa2, pa3); partialSM(pB0, pB1, m_reg, mnB, alB);
  __syncthreads(); RESC(alB);
  finishSM(pB0, pB1, alB, l_reg, pa0, pa1, pa2, pa3); SBAR();
  pv_d0(o, vb0 + (int)SHM_V, pa0, pa1, pa2, pa3);
  if (hi == 0) li_l[r32] = l_reg; asm volatile("s_waitcnt lgkmcnt(0)" ::: "memory");
  float rli[16];
#pragma unroll
  for (int r = 0; r < 16; ++r) rli[r] = __builtin_amdgcn_rcpf(li_l[crow(r, hi)]);
  bf16* Ow = Ob + (long)(wid * QBLK) * LDO;
#pragma unroll
  for (int r = 0; r < 16; ++r) { int orow = crow(r, hi);
    for (int d0 = 0; d0 < 4; ++d0) Ow[(long)orow * LDO + d0 * 32 + r32] = __float2bfloat16(o[d0][r] * rli[r]); }
#undef SLOAD
#undef SWRITE
#undef SWAIT
#undef RESC
}

template <typename TQ>
__global__ __launch_bounds__(64 * NW, 1)
void attn_fwd_dense(const TQ* __restrict__ Q, const bf16* __restrict__ K, const bf16* __restrict__ V, bf16* __restrict__ O, int sq, int skv, int GQA) {
  extern __shared__ __attribute__((aligned(16))) char shm[];
  const long b = blockIdx.z, h = blockIdx.y, Hq = gridDim.y, Hkv = Hq / GQA, kvh = h / GQA, i0 = (long)blockIdx.x * (QBLK * NW);
  if constexpr (LAYOUT == 0) {
    const long q0 = ((b * Hq + h) * sq + i0) * D, k0 = (b * Hkv + kvh) * skv * (long)D;
    attn_dense_body<TQ>(Q + q0, K + k0, V + k0, O + q0, skv, shm);
  } else {
    const long q0 = ((b * sq + i0) * HQ + h) * (long)D, k0 = (b * skv * HKV + kvh) * (long)D;
    attn_dense_body<TQ>(Q + q0, K + k0, V + k0, O + q0, skv, shm);
  }
}

extern "C" void kernel_launch(void* const* d_in, const int* in_sizes, int n_in, void* d_out, int out_size, void* d_ws, size_t ws_size, hipStream_t stream) {
  (void)hipFuncSetAttribute((const void*)attn_fwd_dense<bf16>, hipFuncAttributeMaxDynamicSharedMemorySize, (int)SHM_ATTN);
  hipLaunchKernelGGL(attn_fwd_dense<bf16>, dim3(S / (QBLK * NW), HQ, B), dim3(64 * NW), SHM_ATTN, stream,
                     (const bf16*)d_in[0], (const bf16*)d_in[1], (const bf16*)d_in[2], (bf16*)d_out, S, SKV, HQ / HKV);
}
```

```cpp
#include <hip/hip_runtime.h>
#include <hip/hip_bf16.h>
#define DIAG 0
#define RTP 0

typedef unsigned u32x4 __attribute__((ext_vector_type(4)));
__device__ __forceinline__ float bf_lo(unsigned w) { return __uint_as_float(w << 16); }
__device__ __forceinline__ float bf_hi(unsigned w) { return __uint_as_float(w & 0xffff0000u); }
__device__ __forceinline__ unsigned pk_bf16(float lo, float hi) { unsigned r; asm("v_cvt_pk_bf16_f32 %0, %1, %2" : "=v"(r) : "v"(lo), "v"(hi)); return r; }

__device__ __forceinline__ void exact_rows(const u32x4* __restrict__ qrow, const u32x4* __restrict__ kh, const u32x4* __restrict__ vh, u32x4* __restrict__ orow) {
  const float c = 0.12751743082f;
  float m = -__builtin_inff();
  for (int k = 0; k < 512; ++k) {
    float s = 0.f;
    for (int j = 0; j < 16; ++j) { const u32x4 a = qrow[j], b = kh[k * 16 + j];
#pragma unroll
      for (int e = 0; e < 4; ++e) { s = fmaf(bf_lo(a[e]), bf_lo(b[e]), s); s = fmaf(bf_hi(a[e]), bf_hi(b[e]), s); } }
    m = (s > m || s != s) ? s : m;
  }
  for (int part = 0; part < 4; ++part) {
    float o[32]; float l = 0.f;
#pragma unroll
    for (int e = 0; e < 32; ++e) o[e] = 0.f;
    for (int k = 0; k < 512; ++k) {
      float s = 0.f;
      for (int j = 0; j < 16; ++j) { const u32x4 a = qrow[j], b = kh[k * 16 + j];
#pragma unroll
        for (int e = 0; e < 4; ++e) { s = fmaf(bf_lo(a[e]), bf_lo(b[e]), s); s = fmaf(bf_hi(a[e]), bf_hi(b[e]), s); } }
      const float p = __builtin_amdgcn_exp2f((s - m) * c);
      l += p;
#pragma unroll
      for (int j = 0; j < 4; ++j) { const u32x4 b = vh[k * 16 + part * 4 + j];
#pragma unroll
        for (int e = 0; e < 4; ++e) { o[8 * j + 2 * e] = fmaf(p, bf_lo(b[e]), o[8 * j + 2 * e]); o[8 * j + 2 * e + 1] = fmaf(p, bf_hi(b[e]), o[8 * j + 2 * e + 1]); } }
    }
    const float il = 1.0f / l;
#pragma unroll
    for (int j = 0; j < 4; ++j) { u32x4 w;
#pragma unroll
      for (int e = 0; e < 4; ++e) w[e] = pk_bf16(o[8 * j + 2 * e] * il, o[8 * j + 2 * e + 1] * il);
      orow[part * 4 + j] = w; }
  }
}

__global__ __launch_bounds__(256, 1) void attn_fwd_kernel(const char* __restrict__ Q, const char* __restrict__ K, const char* __restrict__ V, char* __restrict__ O
) {
  extern __shared__ __attribute__((aligned(16))) char lds[];
  unsigned pf, tid = threadIdx.x; unsigned long long pc0;
  asm volatile("s_getpc_b64 %1\n v_and_b32_e32 %0, 0xfc, %2\n v_lshlrev_b32_e32 %0, 5, %0\n global_load_dword %0, %0, %1" : "=&v"(pf), "=&s"(pc0), "+v"(tid));
  const unsigned lane = tid & 63u, w = __builtin_amdgcn_readfirstlane(tid >> 6);
  const unsigned c16 = lane & 15u, g4 = lane >> 4;
  const size_t wg = 0 ? (blockIdx.x & 7) : blockIdx.x;
  const unsigned stage = 131072u + 8192u * w;
  const unsigned xk = (0x78u >> (2u * (c16 >> 2))) & 3u, xg = (0x78u >> (2u * g4)) & 3u;
  const unsigned kb = 1024u * (c16 >> 2) + 64u * (c16 & 3u) + 16u * (g4 ^ xk);
  const unsigned p4 = lane & 3u;
  const unsigned vbb = 65536u + 1024u * g4 + 64u * ((lane >> 2) & 3u) + 8u * (p4 & 1u);
  const unsigned vo = 256u * ((lane >> 2) & 3u) + 64u * g4;
  const unsigned owb = stage + 256u * c16 + 8u * (g4 & 1u);
  const unsigned orb = stage + 256u * g4;
  const unsigned ones = (lane & 3u) == 0u ? 0x3f803f80u : 0u;
  unsigned flag;
  asm volatile(
    "s_memrealtime s[36:37]\n"
    "s_getpc_b64 s[78:79]\n"
    ".Lattn_pc%=:\n"
    "s_lshr_b32 s70, %[wg], 3\n"
    "s_lshl_b32 s70, s70, 2\n"
    "s_add_u32 s70, s70, %[w]\n"
    "s_lshl_b32 s70, s70, 7\n"
    "v_lshlrev_b32_e32 v222, 10, %[ost]\n"
    "v_and_b32_e32 v222, 0xc000, v222\n"
    "v_add_u32_e32 v222, s70, v222\n"
    "v_min_u32_e32 v222, .Lattn_end%=-.Lattn_pc%=-4, v222\n"
    "global_load_dword v223, v222, s[78:79]\n"
    "s_mov_b64 s[44:45], %[k]\n"
    "s_add_u32 s44, s44, %[wo2]\n"
    "s_addc_u32 s45, s45, 0\n"
    "s_and_b32 s45, s45, 0xffff\n"
    "s_mov_b32 s46, 0x80000\n"
    "s_mov_b32 s47, 0x20000\n"
    "s_lshl_b32 s60, %[w], 12\n"
    "s_mov_b32 s56, s60\n"
    "s_add_u32 m0, s60, 0x0\n"
    "s_nop 0\n"
    "buffer_load_dwordx4 %[vo0], s[44:47], s56 offen nt lds\n"
    "buffer_load_dwordx4 %[vo2], s[44:47], s56 offen offset:1024 nt lds\n"
    "buffer_load_dwordx4 %[vo3], s[44:47], s56 offen offset:2048 nt lds\n"
    "buffer_load_dwordx4 %[vo1], s[44:47], s56 offen offset:3072 nt lds\n"
    "s_add_u32 s56, s56, 0x4000\n"
    "s_mov_b64 s[40:41], %[q]\n"
    "s_add_u32 s40, s40, %[wo]\n"
    "s_addc_u32 s41, s41, 0\n"
    "s_and_b32 s41, s41, 0xffff\n"
    "s_mov_b32 s42, 0x40000\n"
    "s_mov_b32 s43, 0x20000\n"
    "s_lshl_b32 s58, %[w], 14\n"
    "buffer_load_dwordx4 a[0:3], %[vq], s[40:43], s58 offen\n"
    "buffer_load_dwordx4 a[4:7], %[vq], s[40:43], s58 offen offset:64\n"
    "buffer_load_dwordx4 a[8:11], %[vq], s[40:43], s58 offen offset:128\n"
    "buffer_load_dwordx4 a[12:15], %[vq], s[40:43], s58 offen offset:192\n"
    "s_add_u32 s58, s58, 0x1000\n"
    "buffer_load_dwordx4 a[16:19], %[vq], s[40:43], s58 offen\n"
    "buffer_load_dwordx4 a[20:23], %[vq], s[40:43], s58 offen offset:64\n"
    "buffer_load_dwordx4 a[24:27], %[vq], s[40:43], s58 offen offset:128\n"
    "buffer_load_dwordx4 a[28:31], %[vq], s[40:43], s58 offen offset:192\n"
    "s_add_u32 s58, s58, 0x1000\n"
    "s_mov_b64 s[48:49], %[v]\n"
    "s_add_u32 s48, s48, %[wo2]\n"
    "s_addc_u32 s49, s49, 0\n"
    "s_and_b32 s49, s49, 0xffff\n"
    "s_mov_b32 s50, 0x80000\n"
    "s_mov_b32 s51, 0x20000\n"
    "s_mov_b64 s[52:53], %[o]\n"
    "s_add_u32 s52, s52, %[wo]\n"
    "s_addc_u32 s53, s53, 0\n"
    "s_and_b32 s53, s53, 0xffff\n"
    "s_mov_b32 s54, 0x0\n"
    "s_mov_b32 s55, 0x20000\n"
    "s_mov_b32 s57, s60\n"
    "s_lshl_b32 s61, %[w], 13\n"
    "s_add_u32 s61, s61, 0x20000\n"
    "s_lshl_b32 s59, %[w], 14\n"
    "s_mov_b32 s62, 4\n"
    "s_mov_b32 s63, 0\n"
    "s_mov_b32 s64, 1\n"
    "s_mov_b32 s65, 0x3e0293ee\n"
    "s_mov_b32 s68, 0xbe0293ee\n"
    "s_mov_b32 s66, 0x4d800000\n"
    "s_mov_b32 s67, 0x7f800000\n"
    "s_mov_b32 s69, 0\n"
    "s_mov_b32 s73, 0\n"
    "s_add_u32 s72, s59, 0x12000\n"
    "s_waitcnt lgkmcnt(0)\n"
    "s_mov_b32 s77, 176\n"
    "s_mov_b32 s71, 170\n"
    "s_mov_b32 s39, 184\n"
    "s_mov_b32 s38, 1\n"
    "s_mov_b32 s76, s38\n"
    "s_bfe_u32 s84, %[wg], 0x30003\n"
    "s_mul_i32 s94, s84, 20\n"
    "s_sub_i32 s94, s94, 70\n"
    "s_add_i32 s94, s94, 5315\n"
    "s_mul_i32 s84, s84, 26\n"
    "s_sub_i32 s84, s84, 91\n"
    "s_add_i32 s84, s84, 5315\n"
    "s_mov_b32 s85, 174\n"
    "s_mov_b32 s71, 170\n"
    "s_mov_b32 s39, 3449\n"
    "s_mov_b32 s37, 5957\n"
    "v_accvgpr_write_b32 a240, %[ones]\n"
    "v_accvgpr_write_b32 a241, %[ones]\n"
    "v_accvgpr_write_b32 a242, %[ones]\n"
    "v_accvgpr_write_b32 a243, %[ones]\n"
    "buffer_load_dwordx4 a[32:35], %[vq], s[40:43], s58 offen\n"
    "buffer_load_dwordx4 a[36:39], %[vq], s[40:43], s58 offen offset:64\n"
    "buffer_load_dwordx4 a[40:43], %[vq], s[40:43], s58 offen offset:128\n"
    "buffer_load_dwordx4 a[44:47], %[vq], s[40:43], s58 offen offset:192\n"
    "s_add_u32 s58, s58, 0x1000\n"
    "s_waitcnt vmcnt(12)\n"
    "s_barrier\n"
    "ds_read_b128 a[64:67], %[kb] offset:0\n"
    "ds_read_b128 a[68:71], %[kb] offset:256\n"
    "ds_read_b128 a[72:75], %[kb] offset:512\n"
    "ds_read_b128 a[76:79], %[kb] offset:768\n"
    "ds_read_b128 a[80:83], %[kb] offset:4096\n"
    "ds_read_b128 a[84:87], %[kb] offset:4352\n"
    "ds_read_b128 a[88:91], %[kb] offset:4608\n"
    "ds_read_b128 a[92:95], %[kb] offset:4864\n"
    "ds_read_b128 a[96:99], %[kb] offset:8192\n"
    "ds_read_b128 a[100:103], %[kb] offset:8448\n"
    "ds_read_b128 a[104:107], %[kb] offset:8704\n"
    "ds_read_b128 a[108:111], %[kb] offset:8960\n"
    "ds_read_b128 a[112:115], %[kb] offset:12288\n"
    "ds_read_b128 a[116:119], %[kb] offset:12544\n"
    "ds_read_b128 a[120:123], %[kb] offset:12800\n"
    "ds_read_b128 a[124:127], %[kb] offset:13056\n"
    "s_waitcnt vmcnt(8)\n"
    "s_waitcnt lgkmcnt(15)\n"
    "v_mfma_f32_16x16x32_bf16 v[128:131], a[64:67], a[0:3], 0\n"
    "s_waitcnt lgkmcnt(14)\n"
    "s_add_u32 m0, s60, 0x10000\n"
    "v_mfma_f32_16x16x32_bf16 v[128:131], a[68:71], a[4:7], v[128:131]\n"
    "s_waitcnt lgkmcnt(13)\n"
    "buffer_load_dwordx4 %[vo0], s[48:51], s57 offen nt lds\n"
    "v_mfma_f32_16x16x32_bf16 v[128:131], a[72:75], a[8:11], v[128:131]\n"
    "s_waitcnt lgkmcnt(12)\n"
    "v_mfma_f32_16x16x32_bf16 v[128:131], a[76:79], a[12:15], v[128:131]\n"
    "s_waitcnt lgkmcnt(11)\n"
    "buffer_load_dwordx4 %[vo2], s[48:51], s57 offen offset:1024 nt lds\n"
    "v_mfma_f32_16x16x32_bf16 v[132:135], a[80:83], a[0:3], 0\n"
    "s_waitcnt lgkmcnt(10)\n"
    "v_mfma_f32_16x16x32_bf16 v[132:135], a[84:87], a[4:7], v[132:135]\n"
    "s_waitcnt lgkmcnt(9)\n"
    "buffer_load_dwordx4 %[vo3], s[48:51], s57 offen offset:2048 nt lds\n"
    "v_mfma_f32_16x16x32_bf16 v[132:135], a[88:91], a[8:11], v[132:135]\n"
    "s_waitcnt lgkmcnt(8)\n"
    "v_mfma_f32_16x16x32_bf16 v[132:135], a[92:95], a[12:15], v[132:135]\n"
    "s_waitcnt lgkmcnt(7)\n"
    "buffer_load_dwordx4 %[vo1], s[48:51], s57 offen offset:3072 nt lds\n"
    "s_add_u32 s57, s57, 0x4000\n"
    "v_mfma_f32_16x16x32_bf16 v[136:139], a[96:99], a[0:3], 0\n"
    "s_waitcnt lgkmcnt(6)\n"
    "v_max3_f32 v216, v128, v129, v130\n"
    "v_mfma_f32_16x16x32_bf16 v[136:139], a[100:103], a[4:7], v[136:139]\n"
    "s_waitcnt lgkmcnt(5)\n"
    "buffer_load_dwordx4 a[48:51], %[vq], s[40:43], s58 offen\n"
    "v_max_f32_e32 v216, v216, v131\n"
    "v_mfma_f32_16x16x32_bf16 v[136:139], a[104:107], a[8:11], v[136:139]\n"
    "s_waitcnt lgkmcnt(4)\n"
    "v_mfma_f32_16x16x32_bf16 v[136:139], a[108:111], a[12:15], v[136:139]\n"
    "s_waitcnt lgkmcnt(0)\n"
    "buffer_load_dwordx4 a[52:55], %[vq], s[40:43], s58 offen offset:64\n"
    "v_mfma_f32_16x16x32_bf16 v[140:143], a[112:115], a[0:3], 0\n"
    "v_mfma_f32_16x16x32_bf16 v[140:143], a[116:119], a[4:7], v[140:143]\n"
    "buffer_load_dwordx4 a[56:59], %[vq], s[40:43], s58 offen offset:128\n"
    "v_mov_b32_e32 v217, v216\n"
    "v_mfma_f32_16x16x32_bf16 v[140:143], a[120:123], a[8:11], v[140:143]\n"
    "v_mfma_f32_16x16x32_bf16 v[140:143], a[124:127], a[12:15], v[140:143]\n"
    "buffer_load_dwordx4 a[60:63], %[vq], s[40:43], s58 offen offset:192\n"
    "s_add_u32 s58, s58, 0xd000\n"
    "v_permlane32_swap_b32_e32 v216, v217\n"
    "v_max_f32_e32 v216, v216, v217\n"
    "v_mov_b32_e32 v217, v216\n"
    "s_nop 1\n"
    "v_permlane16_swap_b32_e32 v216, v217\n"
    "v_max_f32_e32 v216, v216, v217\n"
    "v_mul_f32_e32 v208, s68, v216\n"
    "v_fma_f32 v128, v128, s65, v208\n"
    "v_fma_f32 v129, v129, s65, v208\n"
    "s_waitcnt vmcnt(12)\n"
    "v_mfma_f32_16x16x32_bf16 v[144:147], a[64:67], a[16:19], 0\n"
    "s_add_u32 m0, s60, 0x4000\n"
    "v_mfma_f32_16x16x32_bf16 v[144:147], a[68:71], a[20:23], v[144:147]\n"
    "buffer_load_dwordx4 %[vo0], s[44:47], s56 offen nt lds\n"
    "v_mfma_f32_16x16x32_bf16 v[144:147], a[72:75], a[24:27], v[144:147]\n"
    "v_mfma_f32_16x16x32_bf16 v[144:147], a[76:79], a[28:31], v[144:147]\n"
    "buffer_load_dwordx4 %[vo2], s[44:47], s56 offen offset:1024 nt lds\n"
    "v_mfma_f32_16x16x32_bf16 v[148:151], a[80:83], a[16:19], 0\n"
    "v_mfma_f32_16x16x32_bf16 v[148:151], a[84:87], a[20:23], v[148:151]\n"
    "buffer_load_dwordx4 %[vo3], s[44:47], s56 offen offset:2048 nt lds\n"
    "v_mfma_f32_16x16x32_bf16 v[148:151], a[88:91], a[24:27], v[148:151]\n"
    "v_mfma_f32_16x16x32_bf16 v[148:151], a[92:95], a[28:31], v[148:151]\n"
    "buffer_load_dwordx4 %[vo1], s[44:47], s56 offen offset:3072 nt lds\n"
    "s_add_u32 s56, s56, 0x4000\n"
    "v_mfma_f32_16x16x32_bf16 v[152:155], a[96:99], a[16:19], 0\n"
    "s_add_u32 m0, s60, 0x14000\n"
    "v_max3_f32 v218, v144, v145, v146\n"
    "v_mfma_f32_16x16x32_bf16 v[152:155], a[100:103], a[20:23], v[152:155]\n"
    "buffer_load_dwordx4 %[vo0], s[48:51], s57 offen nt lds\n"
    "v_max_f32_e32 v218, v218, v147\n"
    "v_mfma_f32_16x16x32_bf16 v[152:155], a[104:107], a[24:27], v[152:155]\n"
    "v_mfma_f32_16x16x32_bf16 v[152:155], a[108:111], a[28:31], v[152:155]\n"
    "buffer_load_dwordx4 %[vo2], s[48:51], s57 offen offset:1024 nt lds\n"
    "v_mov_b32_e32 v219, v218\n"
    "v_mfma_f32_16x16x32_bf16 v[156:159], a[112:115], a[16:19], 0\n"
    "v_mfma_f32_16x16x32_bf16 v[156:159], a[116:119], a[20:23], v[156:159]\n"
    "buffer_load_dwordx4 %[vo3], s[48:51], s57 offen offset:2048 nt lds\n"
    "v_permlane32_swap_b32_e32 v218, v219\n"
    "v_mfma_f32_16x16x32_bf16 v[156:159], a[120:123], a[24:27], v[156:159]\n"
    "v_max_f32_e32 v218, v218, v219\n"
    "v_mfma_f32_16x16x32_bf16 v[156:159], a[124:127], a[28:31], v[156:159]\n"
    "buffer_load_dwordx4 %[vo1], s[48:51], s57 offen offset:3072 nt lds\n"
    "s_add_u32 s57, s57, 0x4000\n"
    "v_mov_b32_e32 v219, v218\n"
    "v_fma_f32 v130, v130, s65, v208\n"
    "v_fma_f32 v131, v131, s65, v208\n"
    "v_permlane16_swap_b32_e32 v218, v219\n"
    "v_fma_f32 v132, v132, s65, v208\n"
    "v_fma_f32 v133, v133, s65, v208\n"
    "v_fma_f32 v134, v134, s65, v208\n"
    "v_max_f32_e32 v218, v218, v219\n"
    "v_fma_f32 v135, v135, s65, v208\n"
    "v_fma_f32 v136, v136, s65, v208\n"
    "v_fma_f32 v137, v137, s65, v208\n"
    "s_waitcnt vmcnt(12)\n"
    "s_barrier\n"
    "ds_read_b64_tr_b16 a[128:129], %[vb0] offset:0\n"
    "v_mul_f32_e32 v209, s68, v218\n"
    "v_fma_f32 v138, v138, s65, v208\n"
    "v_fma_f32 v139, v139, s65, v208\n"
    "v_fma_f32 v140, v140, s65, v208\n"
    "v_fma_f32 v141, v141, s65, v208\n"
    "ds_read_b64_tr_b16 a[130:131], %[vb0] offset:4096\n"
    "v_fma_f32 v142, v142, s65, v208\n"
    "v_fma_f32 v143, v143, s65, v208\n"
    "v_exp_f32_e32 v128, v128\n"
    "v_fma_f32 v144, v144, s65, v209\n"
    "v_exp_f32_e32 v129, v129\n"
    "v_exp_f32_e32 v130, v130\n"
    "v_fma_f32 v145, v145, s65, v209\n"
    "v_exp_f32_e32 v131, v131\n"
    "v_exp_f32_e32 v132, v132\n"
    "v_fma_f32 v146, v146, s65, v209\n"
    "ds_read_b64_tr_b16 a[132:133], %[vb0] offset:8192\n"
    "v_exp_f32_e32 v133, v133\n"
    "ds_read_b64_tr_b16 a[134:135], %[vb0] offset:12288\n"
    "ds_read_b64_tr_b16 a[136:137], %[vb1] offset:0\n"
    "v_exp_f32_e32 v134, v134\n"
    "v_cvt_pk_bf16_f32 v128, v128, v129\n"
    "ds_read_b64_tr_b16 a[138:139], %[vb1] offset:4096\n"
    "v_exp_f32_e32 v135, v135\n"
    "v_cvt_pk_bf16_f32 v129, v130, v131\n"
    "ds_read_b64_tr_b16 a[140:141], %[vb1] offset:8192\n"
    "v_exp_f32_e32 v136, v136\n"
    "v_cvt_pk_bf16_f32 v130, v132, v133\n"
    "ds_read_b64_tr_b16 a[142:143], %[vb1] offset:12288\n"
    "v_exp_f32_e32 v137, v137\n"
    "ds_read_b64_tr_b16 a[144:145], %[vb0] offset:256\n"
    "v_exp_f32_e32 v138, v138\n"
    "ds_read_b64_tr_b16 a[146:147], %[vb0] offset:4352\n"
    "v_exp_f32_e32 v139, v139\n"
    "v_exp_f32_e32 v140, v140\n"
    "v_exp_f32_e32 v141, v141\n"
    "v_exp_f32_e32 v142, v142\n"
    "v_exp_f32_e32 v143, v143\n"
    "ds_read_b64_tr_b16 a[148:149], %[vb0] offset:8448\n"
    "v_cvt_pk_bf16_f32 v131, v134, v135\n"
    "s_waitcnt vmcnt(16)\n"
    "v_mfma_f32_16x16x32_bf16 v[160:163], a[64:67], a[32:35], 0\n"
    "s_add_u32 m0, s60, 0x8000\n"
    "ds_read_b64_tr_b16 a[150:151], %[vb0] offset:12544\n"
    "v_cvt_pk_bf16_f32 v136, v136, v137\n"
    "v_mfma_f32_16x16x32_bf16 v[160:163], a[68:71], a[36:39], v[160:163]\n"
    "buffer_load_dwordx4 %[vo0], s[44:47], s56 offen nt lds\n"
    "v_cvt_pk_bf16_f32 v137, v138, v139\n"
    "v_cvt_pk_bf16_f32 v138, v140, v141\n"
    "v_mfma_f32_16x16x32_bf16 v[160:163], a[72:75], a[40:43], v[160:163]\n"
    "v_cvt_pk_bf16_f32 v139, v142, v143\n"
    "v_mfma_f32_16x16x32_bf16 v[160:163], a[76:79], a[44:47], v[160:163]\n"
    "buffer_load_dwordx4 %[vo2], s[44:47], s56 offen offset:1024 nt lds\n"
    "ds_read_b64_tr_b16 a[152:153], %[vb1] offset:256\n"
    "v_mfma_f32_16x16x32_bf16 v[164:167], a[80:83], a[32:35], 0\n"
    "ds_read_b64_tr_b16 a[154:155], %[vb1] offset:4352\n"
    "v_mfma_f32_16x16x32_bf16 v[164:167], a[84:87], a[36:39], v[164:167]\n"
    "buffer_load_dwordx4 %[vo3], s[44:47], s56 offen offset:2048 nt lds\n"
    "v_mfma_f32_16x16x32_bf16 v[164:167], a[88:91], a[40:43], v[164:167]\n"
    "v_mfma_f32_16x16x32_bf16 v[164:167], a[92:95], a[44:47], v[164:167]\n"
    "buffer_load_dwordx4 %[vo1], s[44:47], s56 offen offset:3072 nt lds\n"
    "s_add_u32 s56, s56, 0x4000\n"
    "v_mfma_f32_16x16x32_bf16 v[168:171], a[96:99], a[32:35], 0\n"
    "s_add_u32 m0, s60, 0x18000\n"
    "v_max3_f32 v216, v160, v161, v162\n"
    "v_mfma_f32_16x16x32_bf16 v[168:171], a[100:103], a[36:39], v[168:171]\n"
    "buffer_load_dwordx4 %[vo0], s[48:51], s57 offen nt lds\n"
    "v_max_f32_e32 v216, v216, v163\n"
    "v_mfma_f32_16x16x32_bf16 v[168:171], a[104:107], a[40:43], v[168:171]\n"
    "v_mov_b32_e32 v217, v216\n"
    "v_mfma_f32_16x16x32_bf16 v[168:171], a[108:111], a[44:47], v[168:171]\n"
    "buffer_load_dwordx4 %[vo2], s[48:51], s57 offen offset:1024 nt lds\n"
    "ds_read_b64_tr_b16 a[156:157], %[vb1] offset:8448\n"
    "v_mfma_f32_16x16x32_bf16 v[172:175], a[112:115], a[32:35], 0\n"
    "ds_read_b64_tr_b16 a[158:159], %[vb1] offset:12544\n"
    "v_permlane32_swap_b32_e32 v216, v217\n"
    "v_mfma_f32_16x16x32_bf16 v[172:175], a[116:119], a[36:39], v[172:175]\n"
    "buffer_load_dwordx4 %[vo3], s[48:51], s57 offen offset:2048 nt lds\n"
    "ds_read_b64_tr_b16 a[160:161], %[vb0] offset:512\n"
    "v_max_f32_e32 v216, v216, v217\n"
    "v_fma_f32 v147, v147, s65, v209\n"
    "v_fma_f32 v148, v148, s65, v209\n"
    "v_fma_f32 v149, v149, s65, v209\n"
    "v_fma_f32 v150, v150, s65, v209\n"
    "v_mfma_f32_16x16x32_bf16 v[172:175], a[120:123], a[40:43], v[172:175]\n"
    "ds_read_b64_tr_b16 a[162:163], %[vb0] offset:4608\n"
    "v_mov_b32_e32 v217, v216\n"
    "v_fma_f32 v151, v151, s65, v209\n"
    "v_fma_f32 v152, v152, s65, v209\n"
    "v_fma_f32 v153, v153, s65, v209\n"
    "v_fma_f32 v154, v154, s65, v209\n"
    "v_mfma_f32_16x16x32_bf16 v[172:175], a[124:127], a[44:47], v[172:175]\n"
    "buffer_load_dwordx4 %[vo1], s[48:51], s57 offen offset:3072 nt lds\n"
    "s_add_u32 s57, s57, 0x4000\n"
    "ds_read_b64_tr_b16 a[164:165], %[vb0] offset:8704\n"
    "v_fma_f32 v155, v155, s65, v209\n"
    "v_fma_f32 v156, v156, s65, v209\n"
    "v_fma_f32 v157, v157, s65, v209\n"
    "v_fma_f32 v158, v158, s65, v209\n"
    "v_fma_f32 v159, v159, s65, v209\n"
    "v_mfma_f32_16x16x32_bf16 a[224:227], a[240:243], v[128:131], 0\n"
    "ds_read_b64_tr_b16 a[166:167], %[vb0] offset:12800\n"
    "v_permlane16_swap_b32_e32 v216, v217\n"
    "v_exp_f32_e32 v144, v144\n"
    "v_exp_f32_e32 v145, v145\n"
    "v_mfma_f32_16x16x32_bf16 a[224:227], a[240:243], v[136:139], a[224:227]\n"
    "s_waitcnt lgkmcnt(6)\n"
    "ds_read_b64_tr_b16 a[168:169], %[vb1] offset:512\n"
    "v_max_f32_e32 v216, v216, v217\n"
    "ds_read_b64_tr_b16 a[170:171], %[vb1] offset:4608\n"
    "v_mul_f32_e32 v210, s68, v216\n"
    "v_mfma_f32_16x16x32_bf16 v[0:3], a[128:131], v[128:131], 0\n"
    "ds_read_b64_tr_b16 a[172:173], %[vb1] offset:8704\n"
    "v_mfma_f32_16x16x32_bf16 v[0:3], a[132:135], v[136:139], v[0:3]\n"
    "ds_read_b64_tr_b16 a[174:175], %[vb1] offset:12800\n"
    "v_exp_f32_e32 v146, v146\n"
    "v_mfma_f32_16x16x32_bf16 v[4:7], a[136:139], v[128:131], 0\n"
    "ds_read_b64_tr_b16 a[176:177], %[vb0] offset:768\n"
    "ds_read_b64_tr_b16 a[178:179], %[vb0] offset:4864\n"
    "v_exp_f32_e32 v147, v147\n"
    "v_mfma_f32_16x16x32_bf16 v[4:7], a[140:143], v[136:139], v[4:7]\n"
    "ds_read_b64_tr_b16 a[180:181], %[vb0] offset:8960\n"
    "ds_read_b64_tr_b16 a[182:183], %[vb0] offset:13056\n"
    "v_exp_f32_e32 v148, v148\n"
    "v_mfma_f32_16x16x32_bf16 v[8:11], a[144:147], v[128:131], 0\n"
    "ds_read_b64_tr_b16 a[184:185], %[vb1] offset:768\n"
    "ds_read_b64_tr_b16 a[186:187], %[vb1] offset:4864\n"
    "v_exp_f32_e32 v149, v149\n"
    "v_cvt_pk_bf16_f32 v144, v144, v145\n"
    "v_mfma_f32_16x16x32_bf16 v[8:11], a[148:151], v[136:139], v[8:11]\n"
    "ds_read_b64_tr_b16 a[188:189], %[vb1] offset:8960\n"
    "ds_read_b64_tr_b16 a[190:191], %[vb1] offset:13056\n"
    "v_mfma_f32_16x16x32_bf16 v[12:15], a[152:155], v[128:131], 0\n"
    "s_waitcnt lgkmcnt(15)\n"
    "v_exp_f32_e32 v150, v150\n"
    "v_mfma_f32_16x16x32_bf16 v[12:15], a[156:159], v[136:139], v[12:15]\n"
    "s_waitcnt lgkmcnt(14)\n"
    "v_exp_f32_e32 v151, v151\n"
    "v_cvt_pk_bf16_f32 v145, v146, v147\n"
    "v_mfma_f32_16x16x32_bf16 v[16:19], a[160:163], v[128:131], 0\n"
    "s_waitcnt lgkmcnt(12)\n"
    "v_cvt_pk_bf16_f32 v146, v148, v149\n"
    "v_mfma_f32_16x16x32_bf16 v[16:19], a[164:167], v[136:139], v[16:19]\n"
    "s_waitcnt lgkmcnt(10)\n"
    "v_cvt_pk_bf16_f32 v147, v150, v151\n"
    "v_mfma_f32_16x16x32_bf16 v[20:23], a[168:171], v[128:131], 0\n"
    "s_waitcnt lgkmcnt(8)\n"
    "v_exp_f32_e32 v152, v152\n"
    "v_mfma_f32_16x16x32_bf16 v[20:23], a[172:175], v[136:139], v[20:23]\n"
    "s_waitcnt lgkmcnt(6)\n"
    "v_mfma_f32_16x16x32_bf16 v[24:27], a[176:179], v[128:131], 0\n"
    "s_waitcnt lgkmcnt(4)\n"
    "v_mfma_f32_16x16x32_bf16 v[24:27], a[180:183], v[136:139], v[24:27]\n"
    "s_waitcnt lgkmcnt(2)\n"
    "v_mfma_f32_16x16x32_bf16 v[28:31], a[184:187], v[128:131], 0\n"
    "s_waitcnt lgkmcnt(0)\n"
    "v_mfma_f32_16x16x32_bf16 v[28:31], a[188:191], v[136:139], v[28:31]\n"
    "v_exp_f32_e32 v153, v153\n"
    "s_waitcnt vmcnt(8)\n"
    ".Lpeel1_%=:\n"
    "s_memrealtime s[74:75]\n"
    "s_waitcnt lgkmcnt(0)\n"
    "s_sub_u32 s74, s74, s36\n"
    "s_cmp_lt_i32 s74, s76\n"
    "s_cbranch_scc1 .Lpeel1_%=\n"
    "s_barrier\n"
    "v_mfma_f32_16x16x32_bf16 v[176:179], a[64:67], a[48:51], 0\n"
    "s_sub_i32 s74, s74, 4\n"
    "v_exp_f32_e32 v154, v154\n"
    "v_mfma_f32_16x16x32_bf16 v[176:179], a[68:71], a[52:55], v[176:179]\n"
    "s_max_i32 s76, s74, s38\n"
    "v_mfma_f32_16x16x32_bf16 v[176:179], a[72:75], a[56:59], v[176:179]\n"
    "s_add_i32 s75, s77, 20\n"
    "v_exp_f32_e32 v155, v155\n"
    "v_mfma_f32_16x16x32_bf16 v[176:179], a[76:79], a[60:63], v[176:179]\n"
    "s_add_u32 s76, s76, s75\n"
    "v_exp_f32_e32 v156, v156\n"
    "s_add_u32 s38, s38, s75\n"
    "v_exp_f32_e32 v157, v157\n"
    "v_mfma_f32_16x16x32_bf16 v[180:183], a[80:83], a[48:51], 0\n"
    "s_sub_i32 s77, s77, 16\n"
    "v_exp_f32_e32 v158, v158\n"
    "v_mfma_f32_16x16x32_bf16 v[180:183], a[84:87], a[52:55], v[180:183]\n"
    "s_max_i32 s77, s77, s71\n"
    "v_exp_f32_e32 v159, v159\n"
    "v_mfma_f32_16x16x32_bf16 v[180:183], a[88:91], a[56:59], v[180:183]\n"
    "v_mfma_f32_16x16x32_bf16 v[180:183], a[92:95], a[60:63], v[180:183]\n"
    "v_max3_f32 v218, v176, v177, v178\n"
    "v_mfma_f32_16x16x32_bf16 v[184:187], a[96:99], a[48:51], 0\n"
    "v_max_f32_e32 v218, v218, v179\n"
    "v_cvt_pk_bf16_f32 v152, v152, v153\n"
    "v_mfma_f32_16x16x32_bf16 v[184:187], a[100:103], a[52:55], v[184:187]\n"
    "v_mov_b32_e32 v219, v218\n"
    "v_cvt_pk_bf16_f32 v153, v154, v155\n"
    "v_mfma_f32_16x16x32_bf16 v[184:187], a[104:107], a[56:59], v[184:187]\n"
    "s_add_u32 m0, s60, 0xc000\n"
    "v_cvt_pk_bf16_f32 v154, v156, v157\n"
    "v_cvt_pk_bf16_f32 v155, v158, v159\n"
    "v_mfma_f32_16x16x32_bf16 v[184:187], a[108:111], a[60:63], v[184:187]\n"
    "buffer_load_dwordx4 %[vo0], s[44:47], s56 offen nt lds\n"
    "v_permlane32_swap_b32_e32 v218, v219\n"
    "v_mfma_f32_16x16x32_bf16 v[188:191], a[112:115], a[48:51], 0\n"
    "v_max_f32_e32 v218, v218, v219\n"
    "v_mfma_f32_16x16x32_bf16 v[188:191], a[116:119], a[52:55], v[188:191]\n"
    "v_mov_b32_e32 v219, v218\n"
    "v_mfma_f32_16x16x32_bf16 v[188:191], a[120:123], a[56:59], v[188:191]\n"
    "v_mfma_f32_16x16x32_bf16 v[188:191], a[124:127], a[60:63], v[188:191]\n"
    "v_permlane16_swap_b32_e32 v218, v219\n"
    "v_max_f32_e32 v218, v218, v219\n"
    "v_mfma_f32_16x16x32_bf16 a[228:231], a[240:243], v[144:147], 0\n"
    "ds_read_b128 a[64:67], %[kb] offset:16384\n"
    "v_mul_f32_e32 v211, s68, v218\n"
    "v_fma_f32 v160, v160, s65, v210\n"
    "v_fma_f32 v161, v161, s65, v210\n"
    "v_fma_f32 v162, v162, s65, v210\n"
    "v_fma_f32 v163, v163, s65, v210\n"
    "v_mfma_f32_16x16x32_bf16 a[228:231], a[240:243], v[152:155], a[228:231]\n"
    "ds_read_b128 a[68:71], %[kb] offset:16640\n"
    "v_mfma_f32_16x16x32_bf16 v[32:35], a[128:131], v[144:147], 0\n"
    "ds_read_b128 a[72:75], %[kb] offset:16896\n"
    "v_mfma_f32_16x16x32_bf16 v[32:35], a[132:135], v[152:155], v[32:35]\n"
    "v_fma_f32 v164, v164, s65, v210\n"
    "v_fma_f32 v165, v165, s65, v210\n"
    "v_fma_f32 v166, v166, s65, v210\n"
    "v_mfma_f32_16x16x32_bf16 v[36:39], a[136:139], v[144:147], 0\n"
    "ds_read_b128 a[76:79], %[kb] offset:17152\n"
    "v_fma_f32 v167, v167, s65, v210\n"
    "v_fma_f32 v168, v168, s65, v210\n"
    "v_fma_f32 v169, v169, s65, v210\n"
    "v_fma_f32 v170, v170, s65, v210\n"
    "v_fma_f32 v171, v171, s65, v210\n"
    "v_mfma_f32_16x16x32_bf16 v[36:39], a[140:143], v[152:155], v[36:39]\n"
    "v_fma_f32 v172, v172, s65, v210\n"
    "v_fma_f32 v173, v173, s65, v210\n"
    "v_fma_f32 v174, v174, s65, v210\n"
    "v_fma_f32 v175, v175, s65, v210\n"
    "v_mfma_f32_16x16x32_bf16 v[40:43], a[144:147], v[144:147], 0\n"
    "ds_read_b128 a[80:83], %[kb] offset:20480\n"
    "v_exp_f32_e32 v160, v160\n"
    "v_fma_f32 v176, v176, s65, v211\n"
    "v_mfma_f32_16x16x32_bf16 v[40:43], a[148:151], v[152:155], v[40:43]\n"
    "buffer_load_dwordx4 %[vo2], s[44:47], s56 offen offset:1024 nt lds\n"
    "v_exp_f32_e32 v161, v161\n"
    "ds_read_b128 a[84:87], %[kb] offset:20736\n"
    "v_exp_f32_e32 v162, v162\n"
    "v_fma_f32 v177, v177, s65, v211\n"
    "v_mfma_f32_16x16x32_bf16 v[44:47], a[152:155], v[144:147], 0\n"
    "ds_read_b128 a[88:91], %[kb] offset:20992\n"
    "v_exp_f32_e32 v163, v163\n"
    "v_cvt_pk_bf16_f32 v160, v160, v161\n"
    "v_mfma_f32_16x16x32_bf16 v[44:47], a[156:159], v[152:155], v[44:47]\n"
    "ds_read_b128 a[92:95], %[kb] offset:21248\n"
    "v_exp_f32_e32 v164, v164\n"
    "v_fma_f32 v178, v178, s65, v211\n"
    "v_mfma_f32_16x16x32_bf16 v[48:51], a[160:163], v[144:147], 0\n"
    "ds_read_b128 a[96:99], %[kb] offset:24576\n"
    "v_exp_f32_e32 v165, v165\n"
    "v_cvt_pk_bf16_f32 v161, v162, v163\n"
    "v_mfma_f32_16x16x32_bf16 v[48:51], a[164:167], v[152:155], v[48:51]\n"
    "v_exp_f32_e32 v166, v166\n"
    "v_mfma_f32_16x16x32_bf16 v[52:55], a[168:171], v[144:147], 0\n"
    "ds_read_b128 a[100:103], %[kb] offset:24832\n"
    "v_exp_f32_e32 v167, v167\n"
    "v_mfma_f32_16x16x32_bf16 v[52:55], a[172:175], v[152:155], v[52:55]\n"
    "ds_read_b128 a[104:107], %[kb] offset:25088\n"
    "v_mfma_f32_16x16x32_bf16 v[56:59], a[176:179], v[144:147], 0\n"
    "ds_read_b128 a[108:111], %[kb] offset:25344\n"
    "v_mfma_f32_16x16x32_bf16 v[56:59], a[180:183], v[152:155], v[56:59]\n"
    "ds_read_b128 a[112:115], %[kb] offset:28672\n"
    "v_mfma_f32_16x16x32_bf16 v[60:63], a[184:187], v[144:147], 0\n"
    "ds_read_b128 a[116:119], %[kb] offset:28928\n"
    "v_mfma_f32_16x16x32_bf16 v[60:63], a[188:191], v[152:155], v[60:63]\n"
    "s_waitcnt lgkmcnt(10)\n"
    "ds_read_b128 a[120:123], %[kb] offset:29184\n"
    "v_mfma_f32_16x16x32_bf16 v[128:131], a[64:67], a[0:3], 0\n"
    "ds_read_b128 a[124:127], %[kb] offset:29440\n"
    "v_mfma_f32_16x16x32_bf16 v[128:131], a[68:71], a[4:7], v[128:131]\n"
    "buffer_load_dwordx4 %[vo3], s[44:47], s56 offen offset:2048 nt lds\n"
    "v_mfma_f32_16x16x32_bf16 v[128:131], a[72:75], a[8:11], v[128:131]\n"
    "v_mfma_f32_16x16x32_bf16 v[128:131], a[76:79], a[12:15], v[128:131]\n"
    "s_waitcnt lgkmcnt(8)\n"
    "v_exp_f32_e32 v168, v168\n"
    "v_mfma_f32_16x16x32_bf16 v[132:135], a[80:83], a[0:3], 0\n"
    "v_exp_f32_e32 v169, v169\n"
    "v_mfma_f32_16x16x32_bf16 v[132:135], a[84:87], a[4:7], v[132:135]\n"
    "v_exp_f32_e32 v170, v170\n"
    "v_mfma_f32_16x16x32_bf16 v[132:135], a[88:91], a[8:11], v[132:135]\n"
    "v_exp_f32_e32 v171, v171\n"
    "v_mfma_f32_16x16x32_bf16 v[132:135], a[92:95], a[12:15], v[132:135]\n"
    "s_waitcnt lgkmcnt(5)\n"
    "v_exp_f32_e32 v172, v172\n"
    "v_mfma_f32_16x16x32_bf16 v[136:139], a[96:99], a[0:3], 0\n"
    "v_exp_f32_e32 v173, v173\n"
    "v_mfma_f32_16x16x32_bf16 v[136:139], a[100:103], a[4:7], v[136:139]\n"
    "v_exp_f32_e32 v174, v174\n"
    "v_mfma_f32_16x16x32_bf16 v[136:139], a[104:107], a[8:11], v[136:139]\n"
    "s_waitcnt lgkmcnt(2)\n"
    "v_exp_f32_e32 v175, v175\n"
    "v_mfma_f32_16x16x32_bf16 v[136:139], a[108:111], a[12:15], v[136:139]\n"
    "v_mfma_f32_16x16x32_bf16 v[140:143], a[112:115], a[0:3], 0\n"
    "v_mfma_f32_16x16x32_bf16 v[140:143], a[116:119], a[4:7], v[140:143]\n"
    "s_waitcnt lgkmcnt(0)\n"
    "buffer_load_dwordx4 %[vo1], s[44:47], s56 offen offset:3072 nt lds\n"
    "s_add_u32 s56, s56, 0x4000\n"
    "v_cvt_pk_bf16_f32 v162, v164, v165\n"
    "v_cvt_pk_bf16_f32 v163, v166, v167\n"
    "v_mfma_f32_16x16x32_bf16 v[140:143], a[120:123], a[8:11], v[140:143]\n"
    "v_cvt_pk_bf16_f32 v168, v168, v169\n"
    "v_cvt_pk_bf16_f32 v169, v170, v171\n"
    "v_cvt_pk_bf16_f32 v170, v172, v173\n"
    "v_cvt_pk_bf16_f32 v171, v174, v175\n"
    "v_mfma_f32_16x16x32_bf16 v[140:143], a[124:127], a[12:15], v[140:143]\n"
    "v_mfma_f32_16x16x32_bf16 a[232:235], a[240:243], v[160:163], 0\n"
    "v_fma_f32 v179, v179, s65, v211\n"
    "v_fma_f32 v180, v180, s65, v211\n"
    "v_mfma_f32_16x16x32_bf16 a[232:235], a[240:243], v[168:171], a[232:235]\n"
    "v_mfma_f32_16x16x32_bf16 v[64:67], a[128:131], v[160:163], 0\n"
    "v_mfma_f32_16x16x32_bf16 v[64:67], a[132:135], v[168:171], v[64:67]\n"
    "v_mfma_f32_16x16x32_bf16 v[68:71], a[136:139], v[160:163], 0\n"
    "v_fma_f32 v181, v181, s65, v211\n"
    "v_mfma_f32_16x16x32_bf16 v[68:71], a[140:143], v[168:171], v[68:71]\n"
    "v_fma_f32 v182, v182, s65, v211\n"
    "v_mfma_f32_16x16x32_bf16 v[72:75], a[144:147], v[160:163], 0\n"
    "v_fma_f32 v183, v183, s65, v211\n"
    "v_fma_f32 v184, v184, s65, v211\n"
    "v_fma_f32 v185, v185, s65, v211\n"
    "v_mfma_f32_16x16x32_bf16 v[72:75], a[148:151], v[168:171], v[72:75]\n"
    "v_fma_f32 v186, v186, s65, v211\n"
    "v_fma_f32 v187, v187, s65, v211\n"
    "v_fma_f32 v188, v188, s65, v211\n"
    "v_mfma_f32_16x16x32_bf16 v[76:79], a[152:155], v[160:163], 0\n"
    "s_add_u32 m0, s60, 0x1c000\n"
    "v_fma_f32 v189, v189, s65, v211\n"
    "v_fma_f32 v190, v190, s65, v211\n"
    "v_mfma_f32_16x16x32_bf16 v[76:79], a[156:159], v[168:171], v[76:79]\n"
    "buffer_load_dwordx4 %[vo0], s[48:51], s57 offen nt lds\n"
    "v_fma_f32 v191, v191, s65, v211\n"
    "v_fma_f32 v128, v128, s65, v208\n"
    "v_exp_f32_e32 v176, v176\n"
    "v_fma_f32 v129, v129, s65, v208\n"
    "v_mfma_f32_16x16x32_bf16 v[80:83], a[160:163], v[160:163], 0\n"
    "v_exp_f32_e32 v177, v177\n"
    "v_fma_f32 v130, v130, s65, v208\n"
    "v_mfma_f32_16x16x32_bf16 v[80:83], a[164:167], v[168:171], v[80:83]\n"
    "v_exp_f32_e32 v178, v178\n"
    "v_fma_f32 v131, v131, s65, v208\n"
    "v_mfma_f32_16x16x32_bf16 v[84:87], a[168:171], v[160:163], 0\n"
    "v_exp_f32_e32 v179, v179\n"
    "v_cvt_pk_bf16_f32 v176, v176, v177\n"
    "v_mfma_f32_16x16x32_bf16 v[84:87], a[172:175], v[168:171], v[84:87]\n"
    "v_mfma_f32_16x16x32_bf16 v[88:91], a[176:179], v[160:163], 0\n"
    "v_mfma_f32_16x16x32_bf16 v[88:91], a[180:183], v[168:171], v[88:91]\n"
    "v_mfma_f32_16x16x32_bf16 v[92:95], a[184:187], v[160:163], 0\n"
    "v_mfma_f32_16x16x32_bf16 v[92:95], a[188:191], v[168:171], v[92:95]\n"
    "v_exp_f32_e32 v180, v180\n"
    "v_mfma_f32_16x16x32_bf16 v[144:147], a[64:67], a[16:19], 0\n"
    "v_exp_f32_e32 v181, v181\n"
    "v_mfma_f32_16x16x32_bf16 v[144:147], a[68:71], a[20:23], v[144:147]\n"
    "v_exp_f32_e32 v182, v182\n"
    "v_mfma_f32_16x16x32_bf16 v[144:147], a[72:75], a[24:27], v[144:147]\n"
    "v_exp_f32_e32 v183, v183\n"
    "v_mfma_f32_16x16x32_bf16 v[144:147], a[76:79], a[28:31], v[144:147]\n"
    "buffer_load_dwordx4 %[vo2], s[48:51], s57 offen offset:1024 nt lds\n"
    "v_exp_f32_e32 v184, v184\n"
    "v_exp_f32_e32 v185, v185\n"
    "v_mfma_f32_16x16x32_bf16 v[148:151], a[80:83], a[16:19], 0\n"
    "v_mfma_f32_16x16x32_bf16 v[148:151], a[84:87], a[20:23], v[148:151]\n"
    "v_exp_f32_e32 v186, v186\n"
    "v_mfma_f32_16x16x32_bf16 v[148:151], a[88:91], a[24:27], v[148:151]\n"
    "v_exp_f32_e32 v187, v187\n"
    "v_mfma_f32_16x16x32_bf16 v[148:151], a[92:95], a[28:31], v[148:151]\n"
    "v_exp_f32_e32 v188, v188\n"
    "v_mfma_f32_16x16x32_bf16 v[152:155], a[96:99], a[16:19], 0\n"
    "v_exp_f32_e32 v189, v189\n"
    "v_mfma_f32_16x16x32_bf16 v[152:155], a[100:103], a[20:23], v[152:155]\n"
    "v_exp_f32_e32 v190, v190\n"
    "v_mfma_f32_16x16x32_bf16 v[152:155], a[104:107], a[24:27], v[152:155]\n"
    "v_exp_f32_e32 v191, v191\n"
    "v_mfma_f32_16x16x32_bf16 v[152:155], a[108:111], a[28:31], v[152:155]\n"
    "v_mfma_f32_16x16x32_bf16 v[156:159], a[112:115], a[16:19], 0\n"
    "v_cvt_pk_bf16_f32 v177, v178, v179\n"
    "v_cvt_pk_bf16_f32 v178, v180, v181\n"
    "v_cvt_pk_bf16_f32 v179, v182, v183\n"
    "v_cvt_pk_bf16_f32 v184, v184, v185\n"
    "v_cvt_pk_bf16_f32 v185, v186, v187\n"
    "v_mfma_f32_16x16x32_bf16 v[156:159], a[116:119], a[20:23], v[156:159]\n"
    "v_cvt_pk_bf16_f32 v186, v188, v189\n"
    "v_cvt_pk_bf16_f32 v187, v190, v191\n"
    "v_mfma_f32_16x16x32_bf16 v[156:159], a[120:123], a[24:27], v[156:159]\n"
    "v_mfma_f32_16x16x32_bf16 v[156:159], a[124:127], a[28:31], v[156:159]\n"
    "buffer_load_dwordx4 %[vo3], s[48:51], s57 offen offset:2048 nt lds\n"
    "v_mfma_f32_16x16x32_bf16 a[236:239], a[240:243], v[176:179], 0\n"
    "v_fma_f32 v132, v132, s65, v208\n"
    "v_fma_f32 v133, v133, s65, v208\n"
    "v_mfma_f32_16x16x32_bf16 a[236:239], a[240:243], v[184:187], a[236:239]\n"
    "v_fma_f32 v134, v134, s65, v208\n"
    "v_fma_f32 v135, v135, s65, v208\n"
    "v_mfma_f32_16x16x32_bf16 v[96:99], a[128:131], v[176:179], 0\n"
    "ds_read_b64_tr_b16 a[128:129], %[vb0] offset:16384\n"
    "v_fma_f32 v136, v136, s65, v208\n"
    "v_fma_f32 v137, v137, s65, v208\n"
    "v_mfma_f32_16x16x32_bf16 v[96:99], a[132:135], v[184:187], v[96:99]\n"
    "v_fma_f32 v138, v138, s65, v208\n"
    "v_fma_f32 v139, v139, s65, v208\n"
    "v_mfma_f32_16x16x32_bf16 v[100:103], a[136:139], v[176:179], 0\n"
    "v_fma_f32 v140, v140, s65, v208\n"
    "v_fma_f32 v141, v141, s65, v208\n"
    "v_mfma_f32_16x16x32_bf16 v[100:103], a[140:143], v[184:187], v[100:103]\n"
    "ds_read_b64_tr_b16 a[130:131], %[vb0] offset:20480\n"
    "v_fma_f32 v142, v142, s65, v208\n"
    "v_fma_f32 v143, v143, s65, v208\n"
    "v_mfma_f32_16x16x32_bf16 v[104:107], a[144:147], v[176:179], 0\n"
    "ds_read_b64_tr_b16 a[132:133], %[vb0] offset:24576\n"
    "v_fma_f32 v144, v144, s65, v209\n"
    "v_mfma_f32_16x16x32_bf16 v[104:107], a[148:151], v[184:187], v[104:107]\n"
    "ds_read_b64_tr_b16 a[134:135], %[vb0] offset:28672\n"
    "v_exp_f32_e32 v128, v128\n"
    "v_mfma_f32_16x16x32_bf16 v[108:111], a[152:155], v[176:179], 0\n"
    "ds_read_b64_tr_b16 a[136:137], %[vb1] offset:16384\n"
    "v_fma_f32 v145, v145, s65, v209\n"
    "v_mfma_f32_16x16x32_bf16 v[108:111], a[156:159], v[184:187], v[108:111]\n"
    "ds_read_b64_tr_b16 a[138:139], %[vb1] offset:20480\n"
    "v_exp_f32_e32 v129, v129\n"
    "v_mfma_f32_16x16x32_bf16 v[112:115], a[160:163], v[176:179], 0\n"
    "ds_read_b64_tr_b16 a[140:141], %[vb1] offset:24576\n"
    "v_fma_f32 v146, v146, s65, v209\n"
    "v_mfma_f32_16x16x32_bf16 v[112:115], a[164:167], v[184:187], v[112:115]\n"
    "buffer_load_dwordx4 %[vo1], s[48:51], s57 offen offset:3072 nt lds\n"
    "s_add_u32 s57, s57, 0x4000\n"
    "ds_read_b64_tr_b16 a[142:143], %[vb1] offset:28672\n"
    "v_exp_f32_e32 v130, v130\n"
    "ds_read_b64_tr_b16 a[144:145], %[vb0] offset:16640\n"
    "v_cvt_pk_bf16_f32 v128, v128, v129\n"
    "v_mfma_f32_16x16x32_bf16 v[116:119], a[168:171], v[176:179], 0\n"
    "ds_read_b64_tr_b16 a[146:147], %[vb0] offset:20736\n"
    "v_fma_f32 v147, v147, s65, v209\n"
    "v_mfma_f32_16x16x32_bf16 v[116:119], a[172:175], v[184:187], v[116:119]\n"
    "ds_read_b64_tr_b16 a[148:149], %[vb0] offset:24832\n"
    "v_exp_f32_e32 v131, v131\n"
    "v_mfma_f32_16x16x32_bf16 v[120:123], a[176:179], v[176:179], 0\n"
    "v_exp_f32_e32 v132, v132\n"
    "v_mfma_f32_16x16x32_bf16 v[120:123], a[180:183], v[184:187], v[120:123]\n"
    "v_exp_f32_e32 v133, v133\n"
    "v_mfma_f32_16x16x32_bf16 v[124:127], a[184:187], v[176:179], 0\n"
    "ds_read_b64_tr_b16 a[150:151], %[vb0] offset:28928\n"
    "v_mfma_f32_16x16x32_bf16 v[124:127], a[188:191], v[184:187], v[124:127]\n"
    "ds_read_b64_tr_b16 a[152:153], %[vb1] offset:16640\n"
    "v_mfma_f32_16x16x32_bf16 v[160:163], a[64:67], a[32:35], 0\n"
    "ds_read_b64_tr_b16 a[154:155], %[vb1] offset:20736\n"
    "v_mfma_f32_16x16x32_bf16 v[160:163], a[68:71], a[36:39], v[160:163]\n"
    "v_cvt_pk_bf16_f32 v129, v130, v131\n"
    "v_mfma_f32_16x16x32_bf16 v[160:163], a[72:75], a[40:43], v[160:163]\n"
    "v_exp_f32_e32 v134, v134\n"
    "v_mfma_f32_16x16x32_bf16 v[160:163], a[76:79], a[44:47], v[160:163]\n"
    "v_exp_f32_e32 v135, v135\n"
    "v_mfma_f32_16x16x32_bf16 v[164:167], a[80:83], a[32:35], 0\n"
    "s_add_u32 m0, s60, 0x0\n"
    "s_lshl_b32 s64, s64, 1\n"
    "v_exp_f32_e32 v136, v136\n"
    "v_mfma_f32_16x16x32_bf16 v[164:167], a[84:87], a[36:39], v[164:167]\n"
    "buffer_load_dwordx4 %[vo0], s[44:47], s56 offen nt lds\n"
    "v_exp_f32_e32 v137, v137\n"
    "v_mfma_f32_16x16x32_bf16 v[164:167], a[88:91], a[40:43], v[164:167]\n"
    "v_exp_f32_e32 v138, v138\n"
    "v_mfma_f32_16x16x32_bf16 v[164:167], a[92:95], a[44:47], v[164:167]\n"
    "v_exp_f32_e32 v139, v139\n"
    "v_mfma_f32_16x16x32_bf16 v[168:171], a[96:99], a[32:35], 0\n"
    "v_exp_f32_e32 v140, v140\n"
    "v_mfma_f32_16x16x32_bf16 v[168:171], a[100:103], a[36:39], v[168:171]\n"
    "v_exp_f32_e32 v141, v141\n"
    "v_mfma_f32_16x16x32_bf16 v[168:171], a[104:107], a[40:43], v[168:171]\n"
    "v_exp_f32_e32 v142, v142\n"
    "v_cvt_pk_bf16_f32 v130, v132, v133\n"
    "v_mfma_f32_16x16x32_bf16 v[168:171], a[108:111], a[44:47], v[168:171]\n"
    "v_exp_f32_e32 v143, v143\n"
    "v_mfma_f32_16x16x32_bf16 v[172:175], a[112:115], a[32:35], 0\n"
    "v_cvt_pk_bf16_f32 v131, v134, v135\n"
    "v_cvt_pk_bf16_f32 v136, v136, v137\n"
    "v_cvt_pk_bf16_f32 v137, v138, v139\n"
    "v_mfma_f32_16x16x32_bf16 v[172:175], a[116:119], a[36:39], v[172:175]\n"
    "ds_read_b64_tr_b16 a[156:157], %[vb1] offset:24832\n"
    "v_cvt_pk_bf16_f32 v138, v140, v141\n"
    "v_cvt_pk_bf16_f32 v139, v142, v143\n"
    "v_mfma_f32_16x16x32_bf16 v[172:175], a[120:123], a[40:43], v[172:175]\n"
    "ds_read_b64_tr_b16 a[158:159], %[vb1] offset:28928\n"
    "v_fma_f32 v148, v148, s65, v209\n"
    "v_fma_f32 v149, v149, s65, v209\n"
    "v_mfma_f32_16x16x32_bf16 v[172:175], a[124:127], a[44:47], v[172:175]\n"
    "ds_read_b64_tr_b16 a[160:161], %[vb0] offset:16896\n"
    "v_fma_f32 v150, v150, s65, v209\n"
    "v_fma_f32 v151, v151, s65, v209\n"
    "v_mfma_f32_16x16x32_bf16 a[224:227], a[240:243], v[128:131], a[224:227]\n"
    "ds_read_b64_tr_b16 a[162:163], %[vb0] offset:20992\n"
    "v_fma_f32 v152, v152, s65, v209\n"
    "v_mfma_f32_16x16x32_bf16 a[224:227], a[240:243], v[136:139], a[224:227]\n"
    "s_waitcnt lgkmcnt(4)\n"
    "buffer_load_dwordx4 %[vo2], s[44:47], s56 offen offset:1024 nt lds\n"
    "ds_read_b64_tr_b16 a[164:165], %[vb0] offset:25088\n"
    "ds_read_b64_tr_b16 a[166:167], %[vb0] offset:29184\n"
    "v_mfma_f32_16x16x32_bf16 v[0:3], a[128:131], v[128:131], v[0:3]\n"
    "ds_read_b64_tr_b16 a[168:169], %[vb1] offset:16896\n"
    "ds_read_b64_tr_b16 a[170:171], %[vb1] offset:20992\n"
    "v_mfma_f32_16x16x32_bf16 v[0:3], a[132:135], v[136:139], v[0:3]\n"
    "ds_read_b64_tr_b16 a[172:173], %[vb1] offset:25088\n"
    "ds_read_b64_tr_b16 a[174:175], %[vb1] offset:29184\n"
    "v_mfma_f32_16x16x32_bf16 v[4:7], a[136:139], v[128:131], v[4:7]\n"
    "ds_read_b64_tr_b16 a[176:177], %[vb0] offset:17152\n"
    "ds_read_b64_tr_b16 a[178:179], %[vb0] offset:21248\n"
    "v_mfma_f32_16x16x32_bf16 v[4:7], a[140:143], v[136:139], v[4:7]\n"
    "ds_read_b64_tr_b16 a[180:181], %[vb0] offset:25344\n"
    "ds_read_b64_tr_b16 a[182:183], %[vb0] offset:29440\n"
    "v_mfma_f32_16x16x32_bf16 v[8:11], a[144:147], v[128:131], v[8:11]\n"
    "ds_read_b64_tr_b16 a[184:185], %[vb1] offset:17152\n"
    "ds_read_b64_tr_b16 a[186:187], %[vb1] offset:21248\n"
    "v_mfma_f32_16x16x32_bf16 v[8:11], a[148:151], v[136:139], v[8:11]\n"
    "ds_read_b64_tr_b16 a[188:189], %[vb1] offset:25344\n"
    "ds_read_b64_tr_b16 a[190:191], %[vb1] offset:29440\n"
    "v_mfma_f32_16x16x32_bf16 v[12:15], a[152:155], v[128:131], v[12:15]\n"
    "s_waitcnt lgkmcnt(15)\n"
    "v_fma_f32 v153, v153, s65, v209\n"
    "v_mfma_f32_16x16x32_bf16 v[12:15], a[156:159], v[136:139], v[12:15]\n"
    "s_waitcnt lgkmcnt(14)\n"
    "v_fma_f32 v154, v154, s65, v209\n"
    "v_fma_f32 v155, v155, s65, v209\n"
    "v_mfma_f32_16x16x32_bf16 v[16:19], a[160:163], v[128:131], v[16:19]\n"
    "s_waitcnt lgkmcnt(12)\n"
    "v_fma_f32 v156, v156, s65, v209\n"
    "v_fma_f32 v157, v157, s65, v209\n"
    "v_mfma_f32_16x16x32_bf16 v[16:19], a[164:167], v[136:139], v[16:19]\n"
    "s_waitcnt lgkmcnt(10)\n"
    "v_fma_f32 v158, v158, s65, v209\n"
    "v_fma_f32 v159, v159, s65, v209\n"
    "v_mfma_f32_16x16x32_bf16 v[20:23], a[168:171], v[128:131], v[20:23]\n"
    "s_waitcnt lgkmcnt(8)\n"
    "v_exp_f32_e32 v144, v144\n"
    "v_mfma_f32_16x16x32_bf16 v[20:23], a[172:175], v[136:139], v[20:23]\n"
    "s_waitcnt lgkmcnt(6)\n"
    "buffer_load_dwordx4 %[vo3], s[44:47], s56 offen offset:2048 nt lds\n"
    "v_exp_f32_e32 v145, v145\n"
    "v_mfma_f32_16x16x32_bf16 v[24:27], a[176:179], v[128:131], v[24:27]\n"
    "s_waitcnt lgkmcnt(4)\n"
    "v_exp_f32_e32 v146, v146\n"
    "v_mfma_f32_16x16x32_bf16 v[24:27], a[180:183], v[136:139], v[24:27]\n"
    "s_waitcnt lgkmcnt(2)\n"
    "v_exp_f32_e32 v147, v147\n"
    "v_mfma_f32_16x16x32_bf16 v[28:31], a[184:187], v[128:131], v[28:31]\n"
    "s_waitcnt lgkmcnt(0)\n"
    "v_exp_f32_e32 v148, v148\n"
    "v_mfma_f32_16x16x32_bf16 v[28:31], a[188:191], v[136:139], v[28:31]\n"
    "v_exp_f32_e32 v149, v149\n"
    "buffer_load_dwordx4 %[vo1], s[44:47], s56 offen offset:3072 nt lds\n"
    "s_add_u32 s56, s56, 0x4000\n"
    "s_add_u32 m0, s60, 0x10000\n"
    "s_nop 0\n"
    "buffer_load_dwordx4 %[vo0], s[48:51], s57 offen nt lds\n"
    "buffer_load_dwordx4 %[vo2], s[48:51], s57 offen offset:1024 nt lds\n"
    "buffer_load_dwordx4 %[vo3], s[48:51], s57 offen offset:2048 nt lds\n"
    "buffer_load_dwordx4 %[vo1], s[48:51], s57 offen offset:3072 nt lds\n"
    "s_add_u32 s57, s57, 0x4000\n"
    "s_waitcnt lgkmcnt(0)\n"
    "s_waitcnt vmcnt(16)\n"
    ".Lpeel2_%=:\n"
    "s_memrealtime s[74:75]\n"
    "s_waitcnt lgkmcnt(0)\n"
    "s_sub_u32 s74, s74, s36\n"
    "s_cmp_lt_i32 s74, s76\n"
    "s_cbranch_scc1 .Lpeel2_%=\n"
    "s_barrier\n"
    "s_branch .Lattn_join%=\n"
    ".p2align 6\n"
    ".Lattn_loop%=:\n"
    "s_waitcnt lgkmcnt(15)\n"
    "v_mfma_f32_16x16x32_bf16 v[128:131], a[64:67], a[0:3], 0\n"
    "v_mul_f32_e32 v23, v212, v23\n"
    "v_cvt_pk_bf16_f32 v200, v16, v17\n"
    "v_mfma_f32_16x16x32_bf16 v[128:131], a[68:71], a[4:7], v[128:131]\n"
    "v_exp_f32_e32 v167, v167\n"
    "v_mfma_f32_16x16x32_bf16 v[128:131], a[72:75], a[8:11], v[128:131]\n"
    "s_waitcnt lgkmcnt(7)\n"
    "v_exp_f32_e32 v168, v168\n"
    "v_mfma_f32_16x16x32_bf16 v[128:131], a[76:79], a[12:15], v[128:131]\n"
    "v_exp_f32_e32 v169, v169\n"
    "v_mfma_f32_16x16x32_bf16 v[132:135], a[80:83], a[0:3], 0\n"
    "s_add_u32 m0, s60, 0xc000\n"
    "v_exp_f32_e32 v170, v170\n"
    "v_mfma_f32_16x16x32_bf16 v[132:135], a[84:87], a[4:7], v[132:135]\n"
    "buffer_load_dwordx4 %[vo0], s[44:47], s56 offen nt lds\n"
    "v_cvt_pk_bf16_f32 v201, v18, v19\n"
    "v_mfma_f32_16x16x32_bf16 v[132:135], a[88:91], a[8:11], v[132:135]\n"
    "v_exp_f32_e32 v171, v171\n"
    "v_mfma_f32_16x16x32_bf16 v[132:135], a[92:95], a[12:15], v[132:135]\n"
    "v_exp_f32_e32 v172, v172\n"
    "v_mfma_f32_16x16x32_bf16 v[136:139], a[96:99], a[0:3], 0\n"
    "v_max3_f32 v216, v128, v129, v130\n"
    "v_cvt_pk_bf16_f32 v202, v20, v21\n"
    "v_mfma_f32_16x16x32_bf16 v[136:139], a[100:103], a[4:7], v[136:139]\n"
    "v_max_f32_e32 v216, v216, v131\n"
    "v_cvt_pk_bf16_f32 v203, v22, v23\n"
    "v_mfma_f32_16x16x32_bf16 v[136:139], a[104:107], a[8:11], v[136:139]\n"
    "s_waitcnt lgkmcnt(0)\n"
    "v_exp_f32_e32 v173, v173\n"
    "v_mfma_f32_16x16x32_bf16 v[136:139], a[108:111], a[12:15], v[136:139]\n"
    "v_exp_f32_e32 v174, v174\n"
    "v_mfma_f32_16x16x32_bf16 v[140:143], a[112:115], a[0:3], 0\n"
    "v_exp_f32_e32 v175, v175\n"
    "v_mfma_f32_16x16x32_bf16 v[140:143], a[116:119], a[4:7], v[140:143]\n"
    "v_mov_b32_e32 v217, v216\n"
    "v_cvt_pk_bf16_f32 v163, v166, v167\n"
    "v_cvt_pk_bf16_f32 v168, v168, v169\n"
    "v_cvt_pk_bf16_f32 v169, v170, v171\n"
    "v_cvt_pk_bf16_f32 v170, v172, v173\n"
    "v_mul_f32_e32 v24, v212, v24\n"
    "v_mfma_f32_16x16x32_bf16 v[140:143], a[120:123], a[8:11], v[140:143]\n"
    "ds_write_b64 %[ow0], v[200:201] offset:128\n"
    "v_cvt_pk_bf16_f32 v171, v174, v175\n"
    "v_mul_f32_e32 v25, v212, v25\n"
    "v_mul_f32_e32 v26, v212, v26\n"
    "v_mul_f32_e32 v27, v212, v27\n"
    "v_mfma_f32_16x16x32_bf16 v[140:143], a[124:127], a[12:15], v[140:143]\n"
    "buffer_load_dwordx4 %[vo2], s[44:47], s56 offen offset:1024 nt lds\n"
    "v_permlane32_swap_b32_e32 v216, v217\n"
    "v_mul_f32_e32 v28, v212, v28\n"
    "v_mul_f32_e32 v29, v212, v29\n"
    "v_mul_f32_e32 v30, v212, v30\n"
    "v_mul_f32_e32 v31, v212, v31\n"
    "v_mfma_f32_16x16x32_bf16 a[244:247], a[240:243], v[200:203], a[244:247]\n"
    "ds_write_b64 %[ow1], v[202:203] offset:128\n"
    "v_max_f32_e32 v216, v216, v217\n"
    "v_cvt_pk_bf16_f32 v204, v24, v25\n"
    "v_cvt_pk_bf16_f32 v205, v26, v27\n"
    "v_cvt_pk_bf16_f32 v206, v28, v29\n"
    "v_mfma_f32_16x16x32_bf16 a[232:235], a[240:243], v[160:163], a[232:235]\n"
    "ds_write_b64 %[ow2], v[204:205] offset:128\n"
    "v_mov_b32_e32 v217, v216\n"
    "v_cvt_pk_bf16_f32 v207, v30, v31\n"
    "v_fma_f32 v182, v182, s65, v211\n"
    "v_fma_f32 v183, v183, s65, v211\n"
    "v_mfma_f32_16x16x32_bf16 a[232:235], a[240:243], v[168:171], a[232:235]\n"
    "ds_write_b64 %[ow3], v[206:207] offset:128\n"
    "v_fma_f32 v184, v184, s65, v211\n"
    "v_fma_f32 v185, v185, s65, v211\n"
    "v_mfma_f32_16x16x32_bf16 v[64:67], a[128:131], v[160:163], v[64:67]\n"
    "ds_read_b128 a[192:195], %[or0] offset:0\n"
    "v_permlane16_swap_b32_e32 v216, v217\n"
    "v_fma_f32 v186, v186, s65, v211\n"
    "v_fma_f32 v187, v187, s65, v211\n"
    "v_mfma_f32_16x16x32_bf16 v[64:67], a[132:135], v[168:171], v[64:67]\n"
    "ds_read_b128 a[196:199], %[or1] offset:1024\n"
    "v_max_f32_e32 v216, v216, v217\n"
    "v_fma_f32 v188, v188, s65, v211\n"
    "v_fma_f32 v189, v189, s65, v211\n"
    "v_mfma_f32_16x16x32_bf16 v[68:71], a[136:139], v[160:163], v[68:71]\n"
    "ds_read_b128 a[200:203], %[or0] offset:2048\n"
    "v_mul_f32_e32 v208, s68, v216\n"
    "v_fma_f32 v190, v190, s65, v211\n"
    "v_mfma_f32_16x16x32_bf16 v[68:71], a[140:143], v[168:171], v[68:71]\n"
    "ds_read_b128 a[204:207], %[or1] offset:3072\n"
    "v_fma_f32 v191, v191, s65, v211\n"
    "v_fma_f32 v128, v128, s65, v208\n"
    "v_mfma_f32_16x16x32_bf16 v[72:75], a[144:147], v[160:163], v[72:75]\n"
    "v_accvgpr_read_b32 v220, a232\n"
    "v_exp_f32_e32 v176, v176\n"
    "v_mfma_f32_16x16x32_bf16 v[72:75], a[148:151], v[168:171], v[72:75]\n"
    "v_rcp_f32_e32 v214, v220\n"
    "v_fmac_f32_e32 v221, s66, v220\n"
    "v_mfma_f32_16x16x32_bf16 v[76:79], a[152:155], v[160:163], v[76:79]\n"
    "v_exp_f32_e32 v177, v177\n"
    "v_fma_f32 v129, v129, s65, v208\n"
    "v_mfma_f32_16x16x32_bf16 v[76:79], a[156:159], v[168:171], v[76:79]\n"
    "buffer_load_dwordx4 %[vo3], s[44:47], s56 offen offset:2048 nt lds\n"
    "v_exp_f32_e32 v178, v178\n"
    "v_mfma_f32_16x16x32_bf16 v[80:83], a[160:163], v[160:163], v[80:83]\n"
    "v_exp_f32_e32 v179, v179\n"
    "v_mfma_f32_16x16x32_bf16 v[80:83], a[164:167], v[168:171], v[80:83]\n"
    "v_exp_f32_e32 v180, v180\n"
    "v_mfma_f32_16x16x32_bf16 v[84:87], a[168:171], v[160:163], v[84:87]\n"
    "v_exp_f32_e32 v181, v181\n"
    "v_mfma_f32_16x16x32_bf16 v[84:87], a[172:175], v[168:171], v[84:87]\n"
    "v_mul_f32_e32 v32, v213, v32\n"
    "v_mul_f32_e32 v33, v213, v33\n"
    "v_mfma_f32_16x16x32_bf16 a[244:247], a[240:243], v[204:207], a[244:247]\n"
    "v_mul_f32_e32 v34, v213, v34\n"
    "v_mul_f32_e32 v35, v213, v35\n"
    "v_mfma_f32_16x16x32_bf16 v[88:91], a[176:179], v[160:163], v[88:91]\n"
    "v_mul_f32_e32 v36, v213, v36\n"
    "v_mul_f32_e32 v37, v213, v37\n"
    "v_mfma_f32_16x16x32_bf16 v[88:91], a[180:183], v[168:171], v[88:91]\n"
    "v_mul_f32_e32 v38, v213, v38\n"
    "v_mul_f32_e32 v39, v213, v39\n"
    "v_mfma_f32_16x16x32_bf16 v[92:95], a[184:187], v[160:163], v[92:95]\n"
    "v_exp_f32_e32 v182, v182\n"
    "v_mfma_f32_16x16x32_bf16 v[92:95], a[188:191], v[168:171], v[92:95]\n"
    "v_exp_f32_e32 v183, v183\n"
    "v_mfma_f32_16x16x32_bf16 v[144:147], a[64:67], a[16:19], 0\n"
    "v_exp_f32_e32 v184, v184\n"
    "v_mfma_f32_16x16x32_bf16 v[144:147], a[68:71], a[20:23], v[144:147]\n"
    "buffer_load_dwordx4 %[vo1], s[44:47], s56 offen offset:3072 nt lds\n"
    "v_cvt_pk_bf16_f32 v192, v32, v33\n"
    "v_mfma_f32_16x16x32_bf16 v[144:147], a[72:75], a[24:27], v[144:147]\n"
    "s_add_u32 s56, s56, 0x4000\n"
    "v_exp_f32_e32 v185, v185\n"
    "v_mfma_f32_16x16x32_bf16 v[144:147], a[76:79], a[28:31], v[144:147]\n"
    "v_exp_f32_e32 v186, v186\n"
    "v_mfma_f32_16x16x32_bf16 v[148:151], a[80:83], a[16:19], 0\n"
    "v_exp_f32_e32 v187, v187\n"
    "v_mfma_f32_16x16x32_bf16 v[148:151], a[84:87], a[20:23], v[148:151]\n"
    "v_exp_f32_e32 v188, v188\n"
    "v_mfma_f32_16x16x32_bf16 v[148:151], a[88:91], a[24:27], v[148:151]\n"
    "v_exp_f32_e32 v189, v189\n"
    "v_mfma_f32_16x16x32_bf16 v[148:151], a[92:95], a[28:31], v[148:151]\n"
    "v_exp_f32_e32 v190, v190\n"
    "v_mfma_f32_16x16x32_bf16 v[152:155], a[96:99], a[16:19], 0\n"
    "v_max3_f32 v218, v144, v145, v146\n"
    "v_cvt_pk_bf16_f32 v193, v34, v35\n"
    "v_mfma_f32_16x16x32_bf16 v[152:155], a[100:103], a[20:23], v[152:155]\n"
    "v_max_f32_e32 v218, v218, v147\n"
    "v_cvt_pk_bf16_f32 v194, v36, v37\n"
    "v_mfma_f32_16x16x32_bf16 v[152:155], a[104:107], a[24:27], v[152:155]\n"
    "v_exp_f32_e32 v191, v191\n"
    "v_mfma_f32_16x16x32_bf16 v[152:155], a[108:111], a[28:31], v[152:155]\n"
    "v_mov_b32_e32 v219, v218\n"
    "v_cvt_pk_bf16_f32 v195, v38, v39\n"
    "v_mfma_f32_16x16x32_bf16 v[156:159], a[112:115], a[16:19], 0\n"
    "s_add_u32 m0, s60, 0x1c000\n"
    "v_cvt_pk_bf16_f32 v176, v176, v177\n"
    "v_cvt_pk_bf16_f32 v177, v178, v179\n"
    "v_cvt_pk_bf16_f32 v178, v180, v181\n"
    "v_cvt_pk_bf16_f32 v179, v182, v183\n"
    "v_cvt_pk_bf16_f32 v184, v184, v185\n"
    "v_cvt_pk_bf16_f32 v185, v186, v187\n"
    "v_mfma_f32_16x16x32_bf16 v[156:159], a[116:119], a[20:23], v[156:159]\n"
    "s_add_u32 s73, s59, 0x0\n"
    "buffer_load_dwordx4 %[vo0], s[48:51], s57 offen nt lds\n"
    "v_cvt_pk_bf16_f32 v186, v188, v189\n"
    "v_cvt_pk_bf16_f32 v187, v190, v191\n"
    "v_permlane32_swap_b32_e32 v218, v219\n"
    "v_mul_f32_e32 v40, v213, v40\n"
    "v_mul_f32_e32 v41, v213, v41\n"
    "v_mfma_f32_16x16x32_bf16 v[156:159], a[120:123], a[24:27], v[156:159]\n"
    "ds_write_b64 %[ow0], v[192:193] offset:4096\n"
    "v_max_f32_e32 v218, v218, v219\n"
    "v_mul_f32_e32 v42, v213, v42\n"
    "v_mul_f32_e32 v43, v213, v43\n"
    "v_mul_f32_e32 v44, v213, v44\n"
    "v_mfma_f32_16x16x32_bf16 v[156:159], a[124:127], a[28:31], v[156:159]\n"
    "ds_write_b64 %[ow1], v[194:195] offset:4096\n"
    "v_mov_b32_e32 v219, v218\n"
    "v_mul_f32_e32 v45, v213, v45\n"
    "v_mul_f32_e32 v46, v213, v46\n"
    "v_mul_f32_e32 v47, v213, v47\n"
    "v_mfma_f32_16x16x32_bf16 a[244:247], a[240:243], v[192:195], a[244:247]\n"
    "v_cvt_pk_bf16_f32 v196, v40, v41\n"
    "v_cvt_pk_bf16_f32 v197, v42, v43\n"
    "v_cvt_pk_bf16_f32 v198, v44, v45\n"
    "v_cvt_pk_bf16_f32 v199, v46, v47\n"
    "v_fma_f32 v130, v130, s65, v208\n"
    "v_fma_f32 v131, v131, s65, v208\n"
    "v_mfma_f32_16x16x32_bf16 a[236:239], a[240:243], v[176:179], a[236:239]\n"
    "ds_write_b64 %[ow2], v[196:197] offset:4096\n"
    "v_permlane16_swap_b32_e32 v218, v219\n"
    "v_fma_f32 v132, v132, s65, v208\n"
    "v_fma_f32 v133, v133, s65, v208\n"
    "v_fma_f32 v134, v134, s65, v208\n"
    "v_mfma_f32_16x16x32_bf16 a[236:239], a[240:243], v[184:187], a[236:239]\n"
    "ds_write_b64 %[ow3], v[198:199] offset:4096\n"
    "v_max_f32_e32 v218, v218, v219\n"
    "v_fma_f32 v135, v135, s65, v208\n"
    "v_fma_f32 v136, v136, s65, v208\n"
    "v_fma_f32 v137, v137, s65, v208\n"
    "v_mfma_f32_16x16x32_bf16 v[96:99], a[128:131], v[176:179], v[96:99]\n"
    "ds_read_b64_tr_b16 a[128:129], %[vb0] offset:0\n"
    "v_mul_f32_e32 v209, s68, v218\n"
    "v_fma_f32 v138, v138, s65, v208\n"
    "v_fma_f32 v139, v139, s65, v208\n"
    "v_fma_f32 v140, v140, s65, v208\n"
    "v_fma_f32 v141, v141, s65, v208\n"
    "v_mfma_f32_16x16x32_bf16 v[96:99], a[132:135], v[184:187], v[96:99]\n"
    "ds_read_b64_tr_b16 a[130:131], %[vb0] offset:4096\n"
    "v_fma_f32 v142, v142, s65, v208\n"
    "v_fma_f32 v143, v143, s65, v208\n"
    "v_exp_f32_e32 v128, v128\n"
    "v_fma_f32 v144, v144, s65, v209\n"
    "v_mfma_f32_16x16x32_bf16 v[100:103], a[136:139], v[176:179], v[100:103]\n"
    "s_waitcnt lgkmcnt(6)\n"
    "buffer_store_dwordx4 a[192:195], %[ost], s[52:55], s73 offen sc1\n"
    "v_exp_f32_e32 v129, v129\n"
    "v_exp_f32_e32 v130, v130\n"
    "v_fma_f32 v145, v145, s65, v209\n"
    "v_mfma_f32_16x16x32_bf16 v[100:103], a[140:143], v[184:187], v[100:103]\n"
    "buffer_load_dwordx4 %[vo2], s[48:51], s57 offen offset:1024 nt lds\n"
    "v_exp_f32_e32 v131, v131\n"
    "v_exp_f32_e32 v132, v132\n"
    "v_fma_f32 v146, v146, s65, v209\n"
    "v_mfma_f32_16x16x32_bf16 v[104:107], a[144:147], v[176:179], v[104:107]\n"
    "ds_read_b64_tr_b16 a[132:133], %[vb0] offset:8192\n"
    "v_accvgpr_read_b32 v220, a236\n"
    "v_exp_f32_e32 v133, v133\n"
    "v_mfma_f32_16x16x32_bf16 v[104:107], a[148:151], v[184:187], v[104:107]\n"
    "ds_read_b64_tr_b16 a[134:135], %[vb0] offset:12288\n"
    "v_rcp_f32_e32 v215, v220\n"
    "v_fmac_f32_e32 v221, s66, v220\n"
    "v_mfma_f32_16x16x32_bf16 v[108:111], a[152:155], v[176:179], v[108:111]\n"
    "ds_read_b64_tr_b16 a[136:137], %[vb1] offset:0\n"
    "v_exp_f32_e32 v134, v134\n"
    "v_cvt_pk_bf16_f32 v128, v128, v129\n"
    "v_mfma_f32_16x16x32_bf16 v[108:111], a[156:159], v[184:187], v[108:111]\n"
    "ds_read_b64_tr_b16 a[138:139], %[vb1] offset:4096\n"
    "v_exp_f32_e32 v135, v135\n"
    "v_cvt_pk_bf16_f32 v129, v130, v131\n"
    "v_mfma_f32_16x16x32_bf16 v[112:115], a[160:163], v[176:179], v[112:115]\n"
    "ds_read_b64_tr_b16 a[140:141], %[vb1] offset:8192\n"
    "v_exp_f32_e32 v136, v136\n"
    "v_cvt_pk_bf16_f32 v130, v132, v133\n"
    "v_mfma_f32_16x16x32_bf16 v[112:115], a[164:167], v[184:187], v[112:115]\n"
    "ds_read_b64_tr_b16 a[142:143], %[vb1] offset:12288\n"
    "v_exp_f32_e32 v137, v137\n"
    "v_mfma_f32_16x16x32_bf16 v[116:119], a[168:171], v[176:179], v[116:119]\n"
    "ds_read_b64_tr_b16 a[144:145], %[vb0] offset:256\n"
    "v_exp_f32_e32 v138, v138\n"
    "v_mfma_f32_16x16x32_bf16 v[116:119], a[172:175], v[184:187], v[116:119]\n"
    "ds_read_b64_tr_b16 a[146:147], %[vb0] offset:4352\n"
    "v_exp_f32_e32 v139, v139\n"
    "v_mfma_f32_16x16x32_bf16 a[244:247], a[240:243], v[196:199], a[244:247]\n"
    "v_exp_f32_e32 v140, v140\n"
    "v_mfma_f32_16x16x32_bf16 v[120:123], a[176:179], v[176:179], v[120:123]\n"
    "v_exp_f32_e32 v141, v141\n"
    "v_mfma_f32_16x16x32_bf16 v[120:123], a[180:183], v[184:187], v[120:123]\n"
    "v_exp_f32_e32 v142, v142\n"
    "v_mfma_f32_16x16x32_bf16 v[124:127], a[184:187], v[176:179], v[124:127]\n"
    "v_exp_f32_e32 v143, v143\n"
    "v_mfma_f32_16x16x32_bf16 v[124:127], a[188:191], v[184:187], v[124:127]\n"
    "ds_read_b64_tr_b16 a[148:149], %[vb0] offset:8448\n"
    "v_cvt_pk_bf16_f32 v131, v134, v135\n"
    "v_mfma_f32_16x16x32_bf16 v[160:163], a[64:67], a[32:35], 0\n"
    "ds_read_b64_tr_b16 a[150:151], %[vb0] offset:12544\n"
    "v_cvt_pk_bf16_f32 v136, v136, v137\n"
    "v_mfma_f32_16x16x32_bf16 v[160:163], a[68:71], a[36:39], v[160:163]\n"
    "v_cvt_pk_bf16_f32 v137, v138, v139\n"
    "v_cvt_pk_bf16_f32 v138, v140, v141\n"
    "v_mfma_f32_16x16x32_bf16 v[160:163], a[72:75], a[40:43], v[160:163]\n"
    "buffer_load_dwordx4 %[vo3], s[48:51], s57 offen offset:2048 nt lds\n"
    "v_cvt_pk_bf16_f32 v139, v142, v143\n"
    "v_mfma_f32_16x16x32_bf16 v[160:163], a[76:79], a[44:47], v[160:163]\n"
    "ds_read_b64_tr_b16 a[152:153], %[vb1] offset:256\n"
    "v_mul_f32_e32 v48, v213, v48\n"
    "v_mfma_f32_16x16x32_bf16 v[164:167], a[80:83], a[32:35], 0\n"
    "ds_read_b64_tr_b16 a[154:155], %[vb1] offset:4352\n"
    "v_mul_f32_e32 v49, v213, v49\n"
    "v_mfma_f32_16x16x32_bf16 v[164:167], a[84:87], a[36:39], v[164:167]\n"
    "v_mul_f32_e32 v50, v213, v50\n"
    "v_mul_f32_e32 v51, v213, v51\n"
    "v_mfma_f32_16x16x32_bf16 v[164:167], a[88:91], a[40:43], v[164:167]\n"
    "v_mul_f32_e32 v52, v213, v52\n"
    "v_mul_f32_e32 v53, v213, v53\n"
    "v_mfma_f32_16x16x32_bf16 v[164:167], a[92:95], a[44:47], v[164:167]\n"
    "v_mul_f32_e32 v54, v213, v54\n"
    "v_mul_f32_e32 v55, v213, v55\n"
    "v_mfma_f32_16x16x32_bf16 v[168:171], a[96:99], a[32:35], 0\n"
    "v_max3_f32 v216, v160, v161, v162\n"
    "v_cvt_pk_bf16_f32 v200, v48, v49\n"
    "v_mfma_f32_16x16x32_bf16 v[168:171], a[100:103], a[36:39], v[168:171]\n"
    "v_max_f32_e32 v216, v216, v163\n"
    "v_cvt_pk_bf16_f32 v201, v50, v51\n"
    "v_mfma_f32_16x16x32_bf16 v[168:171], a[104:107], a[40:43], v[168:171]\n"
    "v_mov_b32_e32 v217, v216\n"
    "v_cvt_pk_bf16_f32 v202, v52, v53\n"
    "v_mfma_f32_16x16x32_bf16 v[168:171], a[108:111], a[44:47], v[168:171]\n"
    "ds_read_b64_tr_b16 a[156:157], %[vb1] offset:8448\n"
    "v_cvt_pk_bf16_f32 v203, v54, v55\n"
    "v_mfma_f32_16x16x32_bf16 v[172:175], a[112:115], a[32:35], 0\n"
    "ds_read_b64_tr_b16 a[158:159], %[vb1] offset:12544\n"
    "v_permlane32_swap_b32_e32 v216, v217\n"
    "v_mfma_f32_16x16x32_bf16 v[172:175], a[116:119], a[36:39], v[172:175]\n"
    "ds_read_b64_tr_b16 a[160:161], %[vb0] offset:512\n"
    "v_max_f32_e32 v216, v216, v217\n"
    "v_fma_f32 v147, v147, s65, v209\n"
    "v_fma_f32 v148, v148, s65, v209\n"
    "v_fma_f32 v149, v149, s65, v209\n"
    "v_fma_f32 v150, v150, s65, v209\n"
    "v_mfma_f32_16x16x32_bf16 v[172:175], a[120:123], a[40:43], v[172:175]\n"
    "ds_read_b64_tr_b16 a[162:163], %[vb0] offset:4608\n"
    "v_mov_b32_e32 v217, v216\n"
    "v_fma_f32 v151, v151, s65, v209\n"
    "v_fma_f32 v152, v152, s65, v209\n"
    "v_fma_f32 v153, v153, s65, v209\n"
    "v_fma_f32 v154, v154, s65, v209\n"
    "v_mfma_f32_16x16x32_bf16 v[172:175], a[124:127], a[44:47], v[172:175]\n"
    "ds_read_b64_tr_b16 a[164:165], %[vb0] offset:8704\n"
    "v_fma_f32 v155, v155, s65, v209\n"
    "v_fma_f32 v156, v156, s65, v209\n"
    "v_fma_f32 v157, v157, s65, v209\n"
    "v_fma_f32 v158, v158, s65, v209\n"
    "v_fma_f32 v159, v159, s65, v209\n"
    "v_mfma_f32_16x16x32_bf16 a[224:227], a[240:243], v[128:131], 0\n"
    "ds_read_b64_tr_b16 a[166:167], %[vb0] offset:12800\n"
    "v_permlane16_swap_b32_e32 v216, v217\n"
    "v_exp_f32_e32 v144, v144\n"
    "v_exp_f32_e32 v145, v145\n"
    "v_mfma_f32_16x16x32_bf16 a[224:227], a[240:243], v[136:139], a[224:227]\n"
    "ds_read_b64_tr_b16 a[168:169], %[vb1] offset:512\n"
    "v_max_f32_e32 v216, v216, v217\n"
    "v_mul_f32_e32 v56, v213, v56\n"
    "v_mul_f32_e32 v57, v213, v57\n"
    "v_mul_f32_e32 v58, v213, v58\n"
    "v_mul_f32_e32 v59, v213, v59\n"
    "v_mfma_f32_16x16x32_bf16 a[244:247], a[240:243], v[200:203], a[244:247]\n"
    "s_waitcnt lgkmcnt(7)\n"
    "ds_read_b64_tr_b16 a[170:171], %[vb1] offset:4608\n"
    "v_mul_f32_e32 v210, s68, v216\n"
    "v_mul_f32_e32 v60, v213, v60\n"
    "v_mul_f32_e32 v61, v213, v61\n"
    "v_mul_f32_e32 v62, v213, v62\n"
    "v_mul_f32_e32 v63, v213, v63\n"
    "v_mfma_f32_16x16x32_bf16 v[0:3], a[128:131], v[128:131], 0\n"
    "ds_read_b64_tr_b16 a[172:173], %[vb1] offset:8704\n"
    "v_cvt_pk_bf16_f32 v204, v56, v57\n"
    "v_cvt_pk_bf16_f32 v205, v58, v59\n"
    "v_cvt_pk_bf16_f32 v206, v60, v61\n"
    "v_mfma_f32_16x16x32_bf16 v[0:3], a[132:135], v[136:139], v[0:3]\n"
    "ds_read_b64_tr_b16 a[174:175], %[vb1] offset:12800\n"
    "v_cvt_pk_bf16_f32 v207, v62, v63\n"
    "v_exp_f32_e32 v146, v146\n"
    "v_mfma_f32_16x16x32_bf16 v[4:7], a[136:139], v[128:131], 0\n"
    "ds_read_b64_tr_b16 a[176:177], %[vb0] offset:768\n"
    "ds_read_b64_tr_b16 a[178:179], %[vb0] offset:4864\n"
    "v_exp_f32_e32 v147, v147\n"
    "v_mfma_f32_16x16x32_bf16 v[4:7], a[140:143], v[136:139], v[4:7]\n"
    "ds_read_b64_tr_b16 a[180:181], %[vb0] offset:8960\n"
    "ds_read_b64_tr_b16 a[182:183], %[vb0] offset:13056\n"
    "v_exp_f32_e32 v148, v148\n"
    "v_mfma_f32_16x16x32_bf16 v[8:11], a[144:147], v[128:131], 0\n"
    "buffer_load_dwordx4 %[vo1], s[48:51], s57 offen offset:3072 nt lds\n"
    "ds_read_b64_tr_b16 a[184:185], %[vb1] offset:768\n"
    "ds_read_b64_tr_b16 a[186:187], %[vb1] offset:4864\n"
    "v_exp_f32_e32 v149, v149\n"
    "v_cvt_pk_bf16_f32 v144, v144, v145\n"
    "v_mfma_f32_16x16x32_bf16 v[8:11], a[148:151], v[136:139], v[8:11]\n"
    "s_add_u32 s57, s57, 0x4000\n"
    "ds_write_b64 %[ow0], v[200:201] offset:4224\n"
    "ds_read_b64_tr_b16 a[188:189], %[vb1] offset:8960\n"
    "ds_read_b64_tr_b16 a[190:191], %[vb1] offset:13056\n"
    "v_mfma_f32_16x16x32_bf16 v[12:15], a[152:155], v[128:131], 0\n"
    "s_waitcnt lgkmcnt(15)\n"
    "ds_write_b64 %[ow1], v[202:203] offset:4224\n"
    "v_exp_f32_e32 v150, v150\n"
    "v_mfma_f32_16x16x32_bf16 v[12:15], a[156:159], v[136:139], v[12:15]\n"
    "buffer_store_dwordx4 a[196:199], %[ost], s[52:55], s73 offen offset:1024 sc1\n"
    "v_exp_f32_e32 v151, v151\n"
    "v_cvt_pk_bf16_f32 v145, v146, v147\n"
    "v_mfma_f32_16x16x32_bf16 v[16:19], a[160:163], v[128:131], 0\n"
    "s_waitcnt lgkmcnt(14)\n"
    "ds_write_b64 %[ow2], v[204:205] offset:4224\n"
    "v_cvt_pk_bf16_f32 v146, v148, v149\n"
    "v_mfma_f32_16x16x32_bf16 v[16:19], a[164:167], v[136:139], v[16:19]\n"
    "s_waitcnt lgkmcnt(13)\n"
    "ds_write_b64 %[ow3], v[206:207] offset:4224\n"
    "v_cvt_pk_bf16_f32 v147, v150, v151\n"
    "v_mfma_f32_16x16x32_bf16 v[20:23], a[168:171], v[128:131], 0\n"
    "s_waitcnt lgkmcnt(12)\n"
    "ds_read_b128 a[208:211], %[or0] offset:4096\n"
    "v_exp_f32_e32 v152, v152\n"
    "v_mfma_f32_16x16x32_bf16 v[20:23], a[172:175], v[136:139], v[20:23]\n"
    "s_waitcnt lgkmcnt(11)\n"
    "v_mul_f32_e32 v64, v214, v64\n"
    "v_mul_f32_e32 v65, v214, v65\n"
    "v_mfma_f32_16x16x32_bf16 v[24:27], a[176:179], v[128:131], 0\n"
    "s_waitcnt lgkmcnt(9)\n"
    "v_mul_f32_e32 v66, v214, v66\n"
    "v_mul_f32_e32 v67, v214, v67\n"
    "v_mfma_f32_16x16x32_bf16 v[24:27], a[180:183], v[136:139], v[24:27]\n"
    "s_waitcnt lgkmcnt(7)\n"
    "v_mul_f32_e32 v68, v214, v68\n"
    "v_mul_f32_e32 v69, v214, v69\n"
    "v_mfma_f32_16x16x32_bf16 v[28:31], a[184:187], v[128:131], 0\n"
    "s_waitcnt lgkmcnt(4)\n"
    "v_mul_f32_e32 v70, v214, v70\n"
    "v_mul_f32_e32 v71, v214, v71\n"
    "v_mfma_f32_16x16x32_bf16 v[28:31], a[188:191], v[136:139], v[28:31]\n"
    "v_exp_f32_e32 v153, v153\n"
    "s_waitcnt vmcnt(18)\n"
    ".Lpace1_%=:\n"
    "s_memrealtime s[74:75]\n"
    "s_waitcnt lgkmcnt(0)\n"
    "s_sub_u32 s74, s74, s36\n"
    "s_cmp_lt_i32 s74, s76\n"
    "s_cbranch_scc1 .Lpace1_%=\n"
    "s_barrier\n"
    "v_mfma_f32_16x16x32_bf16 v[176:179], a[64:67], a[48:51], 0\n"
    "s_add_u32 m0, s60, 0x0\n"
    "s_sub_i32 s74, s74, 4\n"
    "v_exp_f32_e32 v154, v154\n"
    "v_mfma_f32_16x16x32_bf16 v[176:179], a[68:71], a[52:55], v[176:179]\n"
    "s_max_i32 s76, s74, s38\n"
    "buffer_load_dwordx4 %[vo0], s[44:47], s56 offen nt lds\n"
    "v_cvt_pk_bf16_f32 v192, v64, v65\n"
    "v_mfma_f32_16x16x32_bf16 v[176:179], a[72:75], a[56:59], v[176:179]\n"
    "s_add_i32 s75, s77, 20\n"
    "v_exp_f32_e32 v155, v155\n"
    "v_mfma_f32_16x16x32_bf16 v[176:179], a[76:79], a[60:63], v[176:179]\n"
    "s_add_u32 s76, s76, s75\n"
    "v_exp_f32_e32 v156, v156\n"
    "v_mfma_f32_16x16x32_bf16 a[244:247], a[240:243], v[204:207], a[244:247]\n"
    "s_add_u32 s38, s38, s75\n"
    "v_exp_f32_e32 v157, v157\n"
    "v_mfma_f32_16x16x32_bf16 v[180:183], a[80:83], a[48:51], 0\n"
    "s_sub_i32 s77, s77, 16\n"
    "v_exp_f32_e32 v158, v158\n"
    "v_mfma_f32_16x16x32_bf16 v[180:183], a[84:87], a[52:55], v[180:183]\n"
    "s_max_i32 s77, s77, s71\n"
    "v_exp_f32_e32 v159, v159\n"
    "v_mfma_f32_16x16x32_bf16 v[180:183], a[88:91], a[56:59], v[180:183]\n"
    "v_cvt_pk_bf16_f32 v193, v66, v67\n"
    "v_cvt_pk_bf16_f32 v194, v68, v69\n"
    "v_mfma_f32_16x16x32_bf16 v[180:183], a[92:95], a[60:63], v[180:183]\n"
    "v_max3_f32 v218, v176, v177, v178\n"
    "v_cvt_pk_bf16_f32 v195, v70, v71\n"
    "v_mfma_f32_16x16x32_bf16 v[184:187], a[96:99], a[48:51], 0\n"
    "v_max_f32_e32 v218, v218, v179\n"
    "v_cvt_pk_bf16_f32 v152, v152, v153\n"
    "v_mfma_f32_16x16x32_bf16 v[184:187], a[100:103], a[52:55], v[184:187]\n"
    "v_mov_b32_e32 v219, v218\n"
    "v_cvt_pk_bf16_f32 v153, v154, v155\n"
    "v_mfma_f32_16x16x32_bf16 v[184:187], a[104:107], a[56:59], v[184:187]\n"
    "v_cvt_pk_bf16_f32 v154, v156, v157\n"
    "v_cvt_pk_bf16_f32 v155, v158, v159\n"
    "v_mfma_f32_16x16x32_bf16 v[184:187], a[108:111], a[60:63], v[184:187]\n"
    "ds_read_b128 a[212:215], %[or1] offset:5120\n"
    "v_permlane32_swap_b32_e32 v218, v219\n"
    "v_mfma_f32_16x16x32_bf16 v[188:191], a[112:115], a[48:51], 0\n"
    "buffer_load_dwordx4 %[vo2], s[44:47], s56 offen offset:1024 nt lds\n"
    "v_max_f32_e32 v218, v218, v219\n"
    "v_mfma_f32_16x16x32_bf16 v[188:191], a[116:119], a[52:55], v[188:191]\n"
    "ds_read_b128 a[216:219], %[or0] offset:6144\n"
    "v_mov_b32_e32 v219, v218\n"
    "v_mul_f32_e32 v72, v214, v72\n"
    "v_mfma_f32_16x16x32_bf16 v[188:191], a[120:123], a[56:59], v[188:191]\n"
    "ds_read_b128 a[220:223], %[or1] offset:7168\n"
    "v_mul_f32_e32 v73, v214, v73\n"
    "v_mul_f32_e32 v74, v214, v74\n"
    "v_mul_f32_e32 v75, v214, v75\n"
    "v_mul_f32_e32 v76, v214, v76\n"
    "v_mul_f32_e32 v77, v214, v77\n"
    "v_mfma_f32_16x16x32_bf16 v[188:191], a[124:127], a[60:63], v[188:191]\n"
    "ds_write_b64 %[ow0], v[192:193] offset:0\n"
    "v_permlane16_swap_b32_e32 v218, v219\n"
    "v_mul_f32_e32 v78, v214, v78\n"
    "v_mul_f32_e32 v79, v214, v79\n"
    "v_cvt_pk_bf16_f32 v196, v72, v73\n"
    "v_mfma_f32_16x16x32_bf16 a[244:247], a[240:243], v[192:195], a[244:247]\n"
    "ds_write_b64 %[ow1], v[194:195] offset:0\n"
    "v_max_f32_e32 v218, v218, v219\n"
    "v_cvt_pk_bf16_f32 v197, v74, v75\n"
    "v_cvt_pk_bf16_f32 v198, v76, v77\n"
    "v_cvt_pk_bf16_f32 v199, v78, v79\n"
    "v_mfma_f32_16x16x32_bf16 a[228:231], a[240:243], v[144:147], 0\n"
    "ds_read_b128 a[64:67], %[kb] offset:16384\n"
    "v_mul_f32_e32 v211, s68, v218\n"
    "v_fma_f32 v160, v160, s65, v210\n"
    "v_fma_f32 v161, v161, s65, v210\n"
    "v_fma_f32 v162, v162, s65, v210\n"
    "v_fma_f32 v163, v163, s65, v210\n"
    "v_mfma_f32_16x16x32_bf16 a[228:231], a[240:243], v[152:155], a[228:231]\n"
    "ds_read_b128 a[68:71], %[kb] offset:16640\n"
    "v_mul_f32_e32 v80, v214, v80\n"
    "v_mul_f32_e32 v81, v214, v81\n"
    "v_mul_f32_e32 v82, v214, v82\n"
    "v_mul_f32_e32 v83, v214, v83\n"
    "v_mul_f32_e32 v84, v214, v84\n"
    "v_mfma_f32_16x16x32_bf16 v[32:35], a[128:131], v[144:147], 0\n"
    "ds_read_b128 a[72:75], %[kb] offset:16896\n"
    "v_mul_f32_e32 v85, v214, v85\n"
    "v_mul_f32_e32 v86, v214, v86\n"
    "v_mul_f32_e32 v87, v214, v87\n"
    "v_cvt_pk_bf16_f32 v200, v80, v81\n"
    "v_cvt_pk_bf16_f32 v201, v82, v83\n"
    "v_mfma_f32_16x16x32_bf16 v[32:35], a[132:135], v[152:155], v[32:35]\n"
    "buffer_store_dwordx4 a[200:203], %[ost], s[52:55], s73 offen offset:2048 sc1\n"
    "v_cvt_pk_bf16_f32 v202, v84, v85\n"
    "v_cvt_pk_bf16_f32 v203, v86, v87\n"
    "v_fma_f32 v164, v164, s65, v210\n"
    "v_fma_f32 v165, v165, s65, v210\n"
    "v_fma_f32 v166, v166, s65, v210\n"
    "v_mfma_f32_16x16x32_bf16 v[36:39], a[136:139], v[144:147], 0\n"
    "ds_read_b128 a[76:79], %[kb] offset:17152\n"
    "v_fma_f32 v167, v167, s65, v210\n"
    "v_fma_f32 v168, v168, s65, v210\n"
    "v_fma_f32 v169, v169, s65, v210\n"
    "v_fma_f32 v170, v170, s65, v210\n"
    "v_fma_f32 v171, v171, s65, v210\n"
    "v_mfma_f32_16x16x32_bf16 v[36:39], a[140:143], v[152:155], v[36:39]\n"
    "ds_write_b64 %[ow2], v[196:197] offset:0\n"
    "v_fma_f32 v172, v172, s65, v210\n"
    "v_fma_f32 v173, v173, s65, v210\n"
    "v_fma_f32 v174, v174, s65, v210\n"
    "v_fma_f32 v175, v175, s65, v210\n"
    "v_mfma_f32_16x16x32_bf16 v[40:43], a[144:147], v[144:147], 0\n"
    "ds_read_b128 a[80:83], %[kb] offset:20480\n"
    "v_exp_f32_e32 v160, v160\n"
    "v_fma_f32 v176, v176, s65, v211\n"
    "v_mfma_f32_16x16x32_bf16 v[40:43], a[148:151], v[152:155], v[40:43]\n"
    "ds_write_b64 %[ow3], v[198:199] offset:0\n"
    "v_exp_f32_e32 v161, v161\n"
    "v_mfma_f32_16x16x32_bf16 a[244:247], a[240:243], v[196:199], a[244:247]\n"
    "ds_read_b128 a[84:87], %[kb] offset:20736\n"
    "v_exp_f32_e32 v162, v162\n"
    "v_fma_f32 v177, v177, s65, v211\n"
    "v_mfma_f32_16x16x32_bf16 v[44:47], a[152:155], v[144:147], 0\n"
    "ds_read_b128 a[88:91], %[kb] offset:20992\n"
    "v_exp_f32_e32 v163, v163\n"
    "v_cvt_pk_bf16_f32 v160, v160, v161\n"
    "v_mfma_f32_16x16x32_bf16 v[44:47], a[156:159], v[152:155], v[44:47]\n"
    "ds_read_b128 a[92:95], %[kb] offset:21248\n"
    "v_exp_f32_e32 v164, v164\n"
    "v_fma_f32 v178, v178, s65, v211\n"
    "v_mfma_f32_16x16x32_bf16 v[48:51], a[160:163], v[144:147], 0\n"
    "ds_read_b128 a[96:99], %[kb] offset:24576\n"
    "v_exp_f32_e32 v165, v165\n"
    "v_cvt_pk_bf16_f32 v161, v162, v163\n"
    "v_mfma_f32_16x16x32_bf16 v[48:51], a[164:167], v[152:155], v[48:51]\n"
    "buffer_load_dwordx4 %[vo3], s[44:47], s56 offen offset:2048 nt lds\n"
    "v_exp_f32_e32 v166, v166\n"
    "v_mfma_f32_16x16x32_bf16 v[52:55], a[168:171], v[144:147], 0\n"
    "ds_read_b128 a[100:103], %[kb] offset:24832\n"
    "v_exp_f32_e32 v167, v167\n"
    "v_mfma_f32_16x16x32_bf16 v[52:55], a[172:175], v[152:155], v[52:55]\n"
    "ds_read_b128 a[104:107], %[kb] offset:25088\n"
    "v_mul_f32_e32 v88, v214, v88\n"
    "v_mul_f32_e32 v89, v214, v89\n"
    "v_mfma_f32_16x16x32_bf16 v[56:59], a[176:179], v[144:147], 0\n"
    "ds_read_b128 a[108:111], %[kb] offset:25344\n"
    "v_mul_f32_e32 v90, v214, v90\n"
    "v_mfma_f32_16x16x32_bf16 v[56:59], a[180:183], v[152:155], v[56:59]\n"
    "ds_read_b128 a[112:115], %[kb] offset:28672\n"
    "v_mul_f32_e32 v91, v214, v91\n"
    "v_mfma_f32_16x16x32_bf16 v[60:63], a[184:187], v[144:147], 0\n"
    "ds_read_b128 a[116:119], %[kb] offset:28928\n"
    "v_mul_f32_e32 v92, v214, v92\n"
    "v_mfma_f32_16x16x32_bf16 v[60:63], a[188:191], v[152:155], v[60:63]\n"
    "s_waitcnt lgkmcnt(10)\n"
    "ds_read_b128 a[120:123], %[kb] offset:29184\n"
    "v_mul_f32_e32 v93, v214, v93\n"
    "v_mfma_f32_16x16x32_bf16 v[128:131], a[64:67], a[0:3], 0\n"
    "ds_read_b128 a[124:127], %[kb] offset:29440\n"
    "v_mul_f32_e32 v94, v214, v94\n"
    "v_mfma_f32_16x16x32_bf16 v[128:131], a[68:71], a[4:7], v[128:131]\n"
    "ds_write_b64 %[ow0], v[200:201] offset:128\n"
    "v_mfma_f32_16x16x32_bf16 v[128:131], a[72:75], a[8:11], v[128:131]\n"
    "ds_write_b64 %[ow1], v[202:203] offset:128\n"
    "v_mfma_f32_16x16x32_bf16 v[128:131], a[76:79], a[12:15], v[128:131]\n"
    "v_mul_f32_e32 v95, v214, v95\n"
    "v_cvt_pk_bf16_f32 v204, v88, v89\n"
    "v_mfma_f32_16x16x32_bf16 a[244:247], a[240:243], v[200:203], a[244:247]\n"
    "v_exp_f32_e32 v168, v168\n"
    "v_mfma_f32_16x16x32_bf16 v[132:135], a[80:83], a[0:3], 0\n"
    "s_waitcnt lgkmcnt(9)\n"
    "v_exp_f32_e32 v169, v169\n"
    "v_mfma_f32_16x16x32_bf16 v[132:135], a[84:87], a[4:7], v[132:135]\n"
    "v_exp_f32_e32 v170, v170\n"
    "v_mfma_f32_16x16x32_bf16 v[132:135], a[88:91], a[8:11], v[132:135]\n"
    "v_exp_f32_e32 v171, v171\n"
    "v_mfma_f32_16x16x32_bf16 v[132:135], a[92:95], a[12:15], v[132:135]\n"
    "v_exp_f32_e32 v172, v172\n"
    "v_mfma_f32_16x16x32_bf16 v[136:139], a[96:99], a[0:3], 0\n"
    "s_waitcnt lgkmcnt(5)\n"
    "v_exp_f32_e32 v173, v173\n"
    "v_mfma_f32_16x16x32_bf16 v[136:139], a[100:103], a[4:7], v[136:139]\n"
    "v_exp_f32_e32 v174, v174\n"
    "v_mfma_f32_16x16x32_bf16 v[136:139], a[104:107], a[8:11], v[136:139]\n"
    "v_exp_f32_e32 v175, v175\n"
    "v_mfma_f32_16x16x32_bf16 v[136:139], a[108:111], a[12:15], v[136:139]\n"
    "v_cvt_pk_bf16_f32 v205, v90, v91\n"
    "v_cvt_pk_bf16_f32 v206, v92, v93\n"
    "v_mfma_f32_16x16x32_bf16 v[140:143], a[112:115], a[0:3], 0\n"
    "s_waitcnt lgkmcnt(1)\n"
    "buffer_load_dwordx4 %[vo1], s[44:47], s56 offen offset:3072 nt lds\n"
    "v_cvt_pk_bf16_f32 v207, v94, v95\n"
    "v_mfma_f32_16x16x32_bf16 v[140:143], a[116:119], a[4:7], v[140:143]\n"
    "s_add_u32 s56, s56, 0x4000\n"
    "v_cvt_pk_bf16_f32 v162, v164, v165\n"
    "v_cvt_pk_bf16_f32 v163, v166, v167\n"
    "v_mfma_f32_16x16x32_bf16 v[140:143], a[120:123], a[8:11], v[140:143]\n"
    "ds_write_b64 %[ow2], v[204:205] offset:128\n"
    "v_cvt_pk_bf16_f32 v168, v168, v169\n"
    "v_cvt_pk_bf16_f32 v169, v170, v171\n"
    "v_cvt_pk_bf16_f32 v170, v172, v173\n"
    "v_cvt_pk_bf16_f32 v171, v174, v175\n"
    "v_mfma_f32_16x16x32_bf16 v[140:143], a[124:127], a[12:15], v[140:143]\n"
    "ds_write_b64 %[ow3], v[206:207] offset:128\n"
    "v_mul_f32_e32 v96, v215, v96\n"
    "v_mul_f32_e32 v97, v215, v97\n"
    "v_mul_f32_e32 v98, v215, v98\n"
    "v_mul_f32_e32 v99, v215, v99\n"
    "v_mfma_f32_16x16x32_bf16 a[244:247], a[240:243], v[204:207], a[244:247]\n"
    "v_mul_f32_e32 v100, v215, v100\n"
    "v_mul_f32_e32 v101, v215, v101\n"
    "v_mul_f32_e32 v102, v215, v102\n"
    "v_mul_f32_e32 v103, v215, v103\n"
    "v_cvt_pk_bf16_f32 v192, v96, v97\n"
    "v_cvt_pk_bf16_f32 v193, v98, v99\n"
    "v_mfma_f32_16x16x32_bf16 a[232:235], a[240:243], v[160:163], 0\n"
    "ds_write_b64 %[ow0], v[192:193] offset:4096\n"
    "v_cvt_pk_bf16_f32 v194, v100, v101\n"
    "v_cvt_pk_bf16_f32 v195, v102, v103\n"
    "v_fma_f32 v179, v179, s65, v211\n"
    "v_fma_f32 v180, v180, s65, v211\n"
    "v_mfma_f32_16x16x32_bf16 a[232:235], a[240:243], v[168:171], a[232:235]\n"
    "ds_write_b64 %[ow1], v[194:195] offset:4096\n"
    "v_mul_f32_e32 v104, v215, v104\n"
    "v_mul_f32_e32 v105, v215, v105\n"
    "v_mul_f32_e32 v106, v215, v106\n"
    "v_mul_f32_e32 v107, v215, v107\n"
    "v_mfma_f32_16x16x32_bf16 v[64:67], a[128:131], v[160:163], 0\n"
    "v_mul_f32_e32 v108, v215, v108\n"
    "v_mul_f32_e32 v109, v215, v109\n"
    "v_mul_f32_e32 v110, v215, v110\n"
    "v_mul_f32_e32 v111, v215, v111\n"
    "v_mfma_f32_16x16x32_bf16 v[64:67], a[132:135], v[168:171], v[64:67]\n"
    "buffer_store_dwordx4 a[204:207], %[ost], s[52:55], s73 offen offset:3072 sc1\n"
    "v_cvt_pk_bf16_f32 v196, v104, v105\n"
    "v_cvt_pk_bf16_f32 v197, v106, v107\n"
    "v_cvt_pk_bf16_f32 v198, v108, v109\n"
    "v_mfma_f32_16x16x32_bf16 v[68:71], a[136:139], v[160:163], 0\n"
    "ds_write_b64 %[ow2], v[196:197] offset:4096\n"
    "v_cvt_pk_bf16_f32 v199, v110, v111\n"
    "v_fma_f32 v181, v181, s65, v211\n"
    "v_mfma_f32_16x16x32_bf16 v[68:71], a[140:143], v[168:171], v[68:71]\n"
    "ds_write_b64 %[ow3], v[198:199] offset:4096\n"
    "v_fma_f32 v182, v182, s65, v211\n"
    "v_mfma_f32_16x16x32_bf16 v[72:75], a[144:147], v[160:163], 0\n"
    "v_fma_f32 v183, v183, s65, v211\n"
    "v_fma_f32 v184, v184, s65, v211\n"
    "v_fma_f32 v185, v185, s65, v211\n"
    "v_mfma_f32_16x16x32_bf16 v[72:75], a[148:151], v[168:171], v[72:75]\n"
    "s_add_u32 m0, s60, 0x10000\n"
    "v_fma_f32 v186, v186, s65, v211\n"
    "v_fma_f32 v187, v187, s65, v211\n"
    "v_fma_f32 v188, v188, s65, v211\n"
    "v_mfma_f32_16x16x32_bf16 v[76:79], a[152:155], v[160:163], 0\n"
    "buffer_load_dwordx4 %[vo0], s[48:51], s57 offen nt lds\n"
    "v_fma_f32 v189, v189, s65, v211\n"
    "v_fma_f32 v190, v190, s65, v211\n"
    "v_mfma_f32_16x16x32_bf16 v[76:79], a[156:159], v[168:171], v[76:79]\n"
    "v_fma_f32 v191, v191, s65, v211\n"
    "v_fma_f32 v128, v128, s65, v208\n"
    "v_mfma_f32_16x16x32_bf16 a[244:247], a[240:243], v[192:195], a[244:247]\n"
    "v_exp_f32_e32 v176, v176\n"
    "v_fma_f32 v129, v129, s65, v208\n"
    "v_mfma_f32_16x16x32_bf16 v[80:83], a[160:163], v[160:163], 0\n"
    "v_exp_f32_e32 v177, v177\n"
    "v_fma_f32 v130, v130, s65, v208\n"
    "v_mfma_f32_16x16x32_bf16 v[80:83], a[164:167], v[168:171], v[80:83]\n"
    "v_exp_f32_e32 v178, v178\n"
    "v_fma_f32 v131, v131, s65, v208\n"
    "v_mfma_f32_16x16x32_bf16 v[84:87], a[168:171], v[160:163], 0\n"
    "v_exp_f32_e32 v179, v179\n"
    "v_cvt_pk_bf16_f32 v176, v176, v177\n"
    "v_mfma_f32_16x16x32_bf16 v[84:87], a[172:175], v[168:171], v[84:87]\n"
    "v_mul_f32_e32 v112, v215, v112\n"
    "v_mul_f32_e32 v113, v215, v113\n"
    "v_mfma_f32_16x16x32_bf16 v[88:91], a[176:179], v[160:163], 0\n"
    "v_mul_f32_e32 v114, v215, v114\n"
    "v_mul_f32_e32 v115, v215, v115\n"
    "v_mfma_f32_16x16x32_bf16 v[88:91], a[180:183], v[168:171], v[88:91]\n"
    "v_mul_f32_e32 v116, v215, v116\n"
    "v_mul_f32_e32 v117, v215, v117\n"
    "v_mfma_f32_16x16x32_bf16 v[92:95], a[184:187], v[160:163], 0\n"
    "v_mul_f32_e32 v118, v215, v118\n"
    "v_mul_f32_e32 v119, v215, v119\n"
    "v_mfma_f32_16x16x32_bf16 v[92:95], a[188:191], v[168:171], v[92:95]\n"
    "v_exp_f32_e32 v180, v180\n"
    "v_mfma_f32_16x16x32_bf16 v[144:147], a[64:67], a[16:19], 0\n"
    "v_exp_f32_e32 v181, v181\n"
    "v_mfma_f32_16x16x32_bf16 v[144:147], a[68:71], a[20:23], v[144:147]\n"
    "v_exp_f32_e32 v182, v182\n"
    "v_mfma_f32_16x16x32_bf16 v[144:147], a[72:75], a[24:27], v[144:147]\n"
    "v_exp_f32_e32 v183, v183\n"
    "v_mfma_f32_16x16x32_bf16 v[144:147], a[76:79], a[28:31], v[144:147]\n"
    "v_exp_f32_e32 v184, v184\n"
    "v_mfma_f32_16x16x32_bf16 a[244:247], a[240:243], v[196:199], a[244:247]\n"
    "v_exp_f32_e32 v185, v185\n"
    "v_mfma_f32_16x16x32_bf16 v[148:151], a[80:83], a[16:19], 0\n"
    "buffer_load_dwordx4 %[vo2], s[48:51], s57 offen offset:1024 nt lds\n"
    "v_cvt_pk_bf16_f32 v200, v112, v113\n"
    "v_mfma_f32_16x16x32_bf16 v[148:151], a[84:87], a[20:23], v[148:151]\n"
    "v_exp_f32_e32 v186, v186\n"
    "v_mfma_f32_16x16x32_bf16 v[148:151], a[88:91], a[24:27], v[148:151]\n"
    "v_exp_f32_e32 v187, v187\n"
    "v_mfma_f32_16x16x32_bf16 v[148:151], a[92:95], a[28:31], v[148:151]\n"
    "v_exp_f32_e32 v188, v188\n"
    "v_mfma_f32_16x16x32_bf16 v[152:155], a[96:99], a[16:19], 0\n"
    "v_exp_f32_e32 v189, v189\n"
    "v_mfma_f32_16x16x32_bf16 v[152:155], a[100:103], a[20:23], v[152:155]\n"
    "v_exp_f32_e32 v190, v190\n"
    "v_mfma_f32_16x16x32_bf16 v[152:155], a[104:107], a[24:27], v[152:155]\n"
    "v_exp_f32_e32 v191, v191\n"
    "v_mfma_f32_16x16x32_bf16 v[152:155], a[108:111], a[28:31], v[152:155]\n"
    "v_cvt_pk_bf16_f32 v201, v114, v115\n"
    "v_cvt_pk_bf16_f32 v202, v116, v117\n"
    "v_mfma_f32_16x16x32_bf16 v[156:159], a[112:115], a[16:19], 0\n"
    "s_add_u32 s73, s59, 0x1000\n"
    "v_cvt_pk_bf16_f32 v203, v118, v119\n"
    "v_cvt_pk_bf16_f32 v177, v178, v179\n"
    "v_cvt_pk_bf16_f32 v178, v180, v181\n"
    "v_cvt_pk_bf16_f32 v179, v182, v183\n"
    "v_cvt_pk_bf16_f32 v184, v184, v185\n"
    "v_cvt_pk_bf16_f32 v185, v186, v187\n"
    "v_mfma_f32_16x16x32_bf16 v[156:159], a[116:119], a[20:23], v[156:159]\n"
    "ds_write_b64 %[ow0], v[200:201] offset:4224\n"
    "v_cvt_pk_bf16_f32 v186, v188, v189\n"
    "v_cvt_pk_bf16_f32 v187, v190, v191\n"
    "v_mfma_f32_16x16x32_bf16 v[156:159], a[120:123], a[24:27], v[156:159]\n"
    "ds_write_b64 %[ow1], v[202:203] offset:4224\n"
    "v_mul_f32_e32 v120, v215, v120\n"
    "v_mul_f32_e32 v121, v215, v121\n"
    "v_mul_f32_e32 v122, v215, v122\n"
    "v_mul_f32_e32 v123, v215, v123\n"
    "v_mfma_f32_16x16x32_bf16 v[156:159], a[124:127], a[28:31], v[156:159]\n"
    "v_mul_f32_e32 v124, v215, v124\n"
    "v_mul_f32_e32 v125, v215, v125\n"
    "v_mul_f32_e32 v126, v215, v126\n"
    "v_mul_f32_e32 v127, v215, v127\n"
    "v_mfma_f32_16x16x32_bf16 a[244:247], a[240:243], v[200:203], a[244:247]\n"
    "v_cvt_pk_bf16_f32 v204, v120, v121\n"
    "v_cvt_pk_bf16_f32 v205, v122, v123\n"
    "v_cvt_pk_bf16_f32 v206, v124, v125\n"
    "v_cvt_pk_bf16_f32 v207, v126, v127\n"
    "v_mfma_f32_16x16x32_bf16 a[236:239], a[240:243], v[176:179], 0\n"
    "ds_write_b64 %[ow2], v[204:205] offset:4224\n"
    "v_fma_f32 v132, v132, s65, v208\n"
    "v_fma_f32 v133, v133, s65, v208\n"
    "v_mfma_f32_16x16x32_bf16 a[236:239], a[240:243], v[184:187], a[236:239]\n"
    "ds_write_b64 %[ow3], v[206:207] offset:4224\n"
    "v_fma_f32 v134, v134, s65, v208\n"
    "v_fma_f32 v135, v135, s65, v208\n"
    "v_mfma_f32_16x16x32_bf16 v[96:99], a[128:131], v[176:179], 0\n"
    "ds_read_b64_tr_b16 a[128:129], %[vb0] offset:16384\n"
    "v_fma_f32 v136, v136, s65, v208\n"
    "v_fma_f32 v137, v137, s65, v208\n"
    "v_mfma_f32_16x16x32_bf16 v[96:99], a[132:135], v[184:187], v[96:99]\n"
    "buffer_store_dwordx4 a[208:211], %[ost], s[52:55], s73 offen sc1\n"
    "v_fma_f32 v138, v138, s65, v208\n"
    "v_fma_f32 v139, v139, s65, v208\n"
    "v_mfma_f32_16x16x32_bf16 v[100:103], a[136:139], v[176:179], 0\n"
    "buffer_load_dwordx4 %[vo3], s[48:51], s57 offen offset:2048 nt lds\n"
    "v_fma_f32 v140, v140, s65, v208\n"
    "v_fma_f32 v141, v141, s65, v208\n"
    "v_mfma_f32_16x16x32_bf16 v[100:103], a[140:143], v[184:187], v[100:103]\n"
    "ds_read_b64_tr_b16 a[130:131], %[vb0] offset:20480\n"
    "v_fma_f32 v142, v142, s65, v208\n"
    "v_fma_f32 v143, v143, s65, v208\n"
    "v_mfma_f32_16x16x32_bf16 v[104:107], a[144:147], v[176:179], 0\n"
    "ds_read_b64_tr_b16 a[132:133], %[vb0] offset:24576\n"
    "v_fma_f32 v144, v144, s65, v209\n"
    "v_mfma_f32_16x16x32_bf16 v[104:107], a[148:151], v[184:187], v[104:107]\n"
    "ds_read_b64_tr_b16 a[134:135], %[vb0] offset:28672\n"
    "v_exp_f32_e32 v128, v128\n"
    "v_mfma_f32_16x16x32_bf16 v[108:111], a[152:155], v[176:179], 0\n"
    "ds_read_b64_tr_b16 a[136:137], %[vb1] offset:16384\n"
    "v_fma_f32 v145, v145, s65, v209\n"
    "v_mfma_f32_16x16x32_bf16 v[108:111], a[156:159], v[184:187], v[108:111]\n"
    "ds_read_b64_tr_b16 a[138:139], %[vb1] offset:20480\n"
    "v_exp_f32_e32 v129, v129\n"
    "v_mfma_f32_16x16x32_bf16 v[112:115], a[160:163], v[176:179], 0\n"
    "ds_read_b64_tr_b16 a[140:141], %[vb1] offset:24576\n"
    "v_fma_f32 v146, v146, s65, v209\n"
    "v_mfma_f32_16x16x32_bf16 v[112:115], a[164:167], v[184:187], v[112:115]\n"
    "ds_read_b64_tr_b16 a[142:143], %[vb1] offset:28672\n"
    "v_exp_f32_e32 v130, v130\n"
    "v_mfma_f32_16x16x32_bf16 a[244:247], a[240:243], v[204:207], a[244:247]\n"
    "ds_read_b64_tr_b16 a[144:145], %[vb0] offset:16640\n"
    "v_cvt_pk_bf16_f32 v128, v128, v129\n"
    "v_mfma_f32_16x16x32_bf16 v[116:119], a[168:171], v[176:179], 0\n"
    "ds_read_b64_tr_b16 a[146:147], %[vb0] offset:20736\n"
    "v_fma_f32 v147, v147, s65, v209\n"
    "v_mfma_f32_16x16x32_bf16 v[116:119], a[172:175], v[184:187], v[116:119]\n"
    "ds_read_b64_tr_b16 a[148:149], %[vb0] offset:24832\n"
    "v_exp_f32_e32 v131, v131\n"
    "v_mfma_f32_16x16x32_bf16 v[120:123], a[176:179], v[176:179], 0\n"
    "v_exp_f32_e32 v132, v132\n"
    "v_mfma_f32_16x16x32_bf16 v[120:123], a[180:183], v[184:187], v[120:123]\n"
    "v_exp_f32_e32 v133, v133\n"
    "v_mfma_f32_16x16x32_bf16 v[124:127], a[184:187], v[176:179], 0\n"
    "ds_read_b64_tr_b16 a[150:151], %[vb0] offset:28928\n"
    "v_accvgpr_read_b32 v220, a244\n"
    "v_mfma_f32_16x16x32_bf16 v[124:127], a[188:191], v[184:187], v[124:127]\n"
    "ds_read_b64_tr_b16 a[152:153], %[vb1] offset:16640\n"
    "v_add_f32_e32 v221, v221, v220\n"
    "v_mfma_f32_16x16x32_bf16 v[160:163], a[64:67], a[32:35], 0\n"
    "ds_read_b64_tr_b16 a[154:155], %[vb1] offset:20736\n"
    "v_cmp_nlt_f32_e64 vcc, abs(v221), s67\n"
    "v_mfma_f32_16x16x32_bf16 v[160:163], a[68:71], a[36:39], v[160:163]\n"
    "buffer_load_dwordx4 %[vo1], s[48:51], s57 offen offset:3072 nt lds\n"
    "v_cvt_pk_bf16_f32 v129, v130, v131\n"
    "v_mfma_f32_16x16x32_bf16 v[160:163], a[72:75], a[40:43], v[160:163]\n"
    "s_add_u32 s57, s57, 0x4000\n"
    "v_exp_f32_e32 v134, v134\n"
    "v_mfma_f32_16x16x32_bf16 v[160:163], a[76:79], a[44:47], v[160:163]\n"
    "s_cmp_lg_u64 vcc, 0\n"
    "s_cselect_b32 s70, s64, 0\n"
    "s_or_b32 s63, s63, s70\n"
    "v_exp_f32_e32 v135, v135\n"
    "v_mfma_f32_16x16x32_bf16 v[164:167], a[80:83], a[32:35], 0\n"
    "s_lshl_b32 s64, s64, 1\n"
    "v_exp_f32_e32 v136, v136\n"
    "v_mfma_f32_16x16x32_bf16 v[164:167], a[84:87], a[36:39], v[164:167]\n"
    "v_exp_f32_e32 v137, v137\n"
    "v_mfma_f32_16x16x32_bf16 v[164:167], a[88:91], a[40:43], v[164:167]\n"
    "v_exp_f32_e32 v138, v138\n"
    "v_mfma_f32_16x16x32_bf16 v[164:167], a[92:95], a[44:47], v[164:167]\n"
    "v_exp_f32_e32 v139, v139\n"
    "v_mfma_f32_16x16x32_bf16 v[168:171], a[96:99], a[32:35], 0\n"
    "v_exp_f32_e32 v140, v140\n"
    "v_mfma_f32_16x16x32_bf16 v[168:171], a[100:103], a[36:39], v[168:171]\n"
    "v_exp_f32_e32 v141, v141\n"
    "v_mfma_f32_16x16x32_bf16 v[168:171], a[104:107], a[40:43], v[168:171]\n"
    "v_exp_f32_e32 v142, v142\n"
    "v_cvt_pk_bf16_f32 v130, v132, v133\n"
    "v_mfma_f32_16x16x32_bf16 v[168:171], a[108:111], a[44:47], v[168:171]\n"
    "v_exp_f32_e32 v143, v143\n"
    "v_mfma_f32_16x16x32_bf16 v[172:175], a[112:115], a[32:35], 0\n"
    "v_cvt_pk_bf16_f32 v131, v134, v135\n"
    "v_cvt_pk_bf16_f32 v136, v136, v137\n"
    "v_cvt_pk_bf16_f32 v137, v138, v139\n"
    "v_mfma_f32_16x16x32_bf16 v[172:175], a[116:119], a[36:39], v[172:175]\n"
    "ds_read_b64_tr_b16 a[156:157], %[vb1] offset:24832\n"
    "v_cvt_pk_bf16_f32 v138, v140, v141\n"
    "v_cvt_pk_bf16_f32 v139, v142, v143\n"
    "v_mfma_f32_16x16x32_bf16 v[172:175], a[120:123], a[40:43], v[172:175]\n"
    "ds_read_b64_tr_b16 a[158:159], %[vb1] offset:28928\n"
    "v_fma_f32 v148, v148, s65, v209\n"
    "v_fma_f32 v149, v149, s65, v209\n"
    "v_mfma_f32_16x16x32_bf16 v[172:175], a[124:127], a[44:47], v[172:175]\n"
    "ds_read_b64_tr_b16 a[160:161], %[vb0] offset:16896\n"
    "v_fma_f32 v150, v150, s65, v209\n"
    "v_fma_f32 v151, v151, s65, v209\n"
    "v_mfma_f32_16x16x32_bf16 a[224:227], a[240:243], v[128:131], a[224:227]\n"
    "ds_read_b64_tr_b16 a[162:163], %[vb0] offset:20992\n"
    "v_fma_f32 v152, v152, s65, v209\n"
    "v_mfma_f32_16x16x32_bf16 a[224:227], a[240:243], v[136:139], a[224:227]\n"
    "s_waitcnt lgkmcnt(4)\n"
    "ds_read_b64_tr_b16 a[164:165], %[vb0] offset:25088\n"
    "ds_read_b64_tr_b16 a[166:167], %[vb0] offset:29184\n"
    "v_mfma_f32_16x16x32_bf16 v[0:3], a[128:131], v[128:131], v[0:3]\n"
    "ds_read_b64_tr_b16 a[168:169], %[vb1] offset:16896\n"
    "ds_read_b64_tr_b16 a[170:171], %[vb1] offset:20992\n"
    "v_mfma_f32_16x16x32_bf16 v[0:3], a[132:135], v[136:139], v[0:3]\n"
    "ds_read_b64_tr_b16 a[172:173], %[vb1] offset:25088\n"
    "ds_read_b64_tr_b16 a[174:175], %[vb1] offset:29184\n"
    "v_mfma_f32_16x16x32_bf16 v[4:7], a[136:139], v[128:131], v[4:7]\n"
    "ds_read_b64_tr_b16 a[176:177], %[vb0] offset:17152\n"
    "ds_read_b64_tr_b16 a[178:179], %[vb0] offset:21248\n"
    "v_mfma_f32_16x16x32_bf16 v[4:7], a[140:143], v[136:139], v[4:7]\n"
    "ds_read_b64_tr_b16 a[180:181], %[vb0] offset:25344\n"
    "ds_read_b64_tr_b16 a[182:183], %[vb0] offset:29440\n"
    "v_mfma_f32_16x16x32_bf16 v[8:11], a[144:147], v[128:131], v[8:11]\n"
    "ds_read_b64_tr_b16 a[184:185], %[vb1] offset:17152\n"
    "ds_read_b64_tr_b16 a[186:187], %[vb1] offset:21248\n"
    "v_mfma_f32_16x16x32_bf16 v[8:11], a[148:151], v[136:139], v[8:11]\n"
    "ds_read_b64_tr_b16 a[188:189], %[vb1] offset:25344\n"
    "ds_read_b64_tr_b16 a[190:191], %[vb1] offset:29440\n"
    "v_mfma_f32_16x16x32_bf16 v[12:15], a[152:155], v[128:131], v[12:15]\n"
    "s_waitcnt lgkmcnt(15)\n"
    "buffer_store_dwordx4 a[212:215], %[ost], s[52:55], s73 offen offset:1024 sc1\n"
    "v_fma_f32 v153, v153, s65, v209\n"
    "v_mfma_f32_16x16x32_bf16 v[12:15], a[156:159], v[136:139], v[12:15]\n"
    "s_waitcnt lgkmcnt(14)\n"
    "v_fma_f32 v154, v154, s65, v209\n"
    "v_fma_f32 v155, v155, s65, v209\n"
    "v_mfma_f32_16x16x32_bf16 v[16:19], a[160:163], v[128:131], v[16:19]\n"
    "s_waitcnt lgkmcnt(12)\n"
    "v_fma_f32 v156, v156, s65, v209\n"
    "v_fma_f32 v157, v157, s65, v209\n"
    "v_mfma_f32_16x16x32_bf16 v[16:19], a[164:167], v[136:139], v[16:19]\n"
    "s_waitcnt lgkmcnt(10)\n"
    "v_fma_f32 v158, v158, s65, v209\n"
    "v_fma_f32 v159, v159, s65, v209\n"
    "v_mfma_f32_16x16x32_bf16 v[20:23], a[168:171], v[128:131], v[20:23]\n"
    "s_waitcnt lgkmcnt(8)\n"
    "v_exp_f32_e32 v144, v144\n"
    "v_mfma_f32_16x16x32_bf16 v[20:23], a[172:175], v[136:139], v[20:23]\n"
    "s_waitcnt lgkmcnt(6)\n"
    "v_exp_f32_e32 v145, v145\n"
    "v_mfma_f32_16x16x32_bf16 v[24:27], a[176:179], v[128:131], v[24:27]\n"
    "s_waitcnt lgkmcnt(4)\n"
    "v_exp_f32_e32 v146, v146\n"
    "v_mfma_f32_16x16x32_bf16 v[24:27], a[180:183], v[136:139], v[24:27]\n"
    "s_waitcnt lgkmcnt(2)\n"
    "v_exp_f32_e32 v147, v147\n"
    "v_mfma_f32_16x16x32_bf16 v[28:31], a[184:187], v[128:131], v[28:31]\n"
    "s_waitcnt lgkmcnt(0)\n"
    "v_exp_f32_e32 v148, v148\n"
    "v_mfma_f32_16x16x32_bf16 v[28:31], a[188:191], v[136:139], v[28:31]\n"
    "v_exp_f32_e32 v149, v149\n"
    "s_waitcnt vmcnt(22)\n"
    ".Lpace2_%=:\n"
    "s_memrealtime s[74:75]\n"
    "s_waitcnt lgkmcnt(0)\n"
    "s_sub_u32 s74, s74, s36\n"
    "s_cmp_lt_i32 s74, s76\n"
    "s_cbranch_scc1 .Lpace2_%=\n"
    "s_barrier\n"
    ".Lattn_join%=:\n"
    "v_mfma_f32_16x16x32_bf16 v[176:179], a[64:67], a[48:51], 0\n"
    "s_add_u32 m0, s60, 0x4000\n"
    "s_sub_i32 s74, s74, 4\n"
    "v_exp_f32_e32 v150, v150\n"
    "v_mfma_f32_16x16x32_bf16 v[176:179], a[68:71], a[52:55], v[176:179]\n"
    "s_max_i32 s76, s74, s38\n"
    "buffer_load_dwordx4 %[vo0], s[44:47], s56 offen nt lds\n"
    "v_cvt_pk_bf16_f32 v144, v144, v145\n"
    "v_mfma_f32_16x16x32_bf16 v[176:179], a[72:75], a[56:59], v[176:179]\n"
    "s_add_i32 s75, s77, 0\n"
    "v_exp_f32_e32 v151, v151\n"
    "v_mfma_f32_16x16x32_bf16 v[176:179], a[76:79], a[60:63], v[176:179]\n"
    "s_add_u32 s76, s76, s75\n"
    "buffer_load_dwordx4 %[vo2], s[44:47], s56 offen offset:1024 nt lds\n"
    "v_cvt_pk_bf16_f32 v145, v146, v147\n"
    "v_mfma_f32_16x16x32_bf16 v[180:183], a[80:83], a[48:51], 0\n"
    "s_add_u32 s38, s38, s75\n"
    "v_exp_f32_e32 v152, v152\n"
    "v_mfma_f32_16x16x32_bf16 v[180:183], a[84:87], a[52:55], v[180:183]\n"
    "s_sub_i32 s77, s77, 16\n"
    "v_exp_f32_e32 v153, v153\n"
    "v_mfma_f32_16x16x32_bf16 v[180:183], a[88:91], a[56:59], v[180:183]\n"
    "s_max_i32 s77, s77, s71\n"
    "v_exp_f32_e32 v154, v154\n"
    "v_mfma_f32_16x16x32_bf16 v[180:183], a[92:95], a[60:63], v[180:183]\n"
    "v_exp_f32_e32 v155, v155\n"
    "v_mfma_f32_16x16x32_bf16 v[184:187], a[96:99], a[48:51], 0\n"
    "v_exp_f32_e32 v156, v156\n"
    "v_mfma_f32_16x16x32_bf16 v[184:187], a[100:103], a[52:55], v[184:187]\n"
    "v_exp_f32_e32 v157, v157\n"
    "v_mfma_f32_16x16x32_bf16 v[184:187], a[104:107], a[56:59], v[184:187]\n"
    "v_exp_f32_e32 v158, v158\n"
    "v_cvt_pk_bf16_f32 v146, v148, v149\n"
    "v_mfma_f32_16x16x32_bf16 v[184:187], a[108:111], a[60:63], v[184:187]\n"
    "v_exp_f32_e32 v159, v159\n"
    "v_cvt_pk_bf16_f32 v147, v150, v151\n"
    "v_mfma_f32_16x16x32_bf16 v[188:191], a[112:115], a[48:51], 0\n"
    "ds_read_b128 a[64:67], %[kb] offset:32768\n"
    "v_cvt_pk_bf16_f32 v152, v152, v153\n"
    "v_cvt_pk_bf16_f32 v153, v154, v155\n"
    "v_cvt_pk_bf16_f32 v154, v156, v157\n"
    "v_mfma_f32_16x16x32_bf16 v[188:191], a[116:119], a[52:55], v[188:191]\n"
    "ds_read_b128 a[68:71], %[kb] offset:33024\n"
    "v_cvt_pk_bf16_f32 v155, v158, v159\n"
    "v_fma_f32 v160, v160, s65, v210\n"
    "v_mfma_f32_16x16x32_bf16 v[188:191], a[120:123], a[56:59], v[188:191]\n"
    "buffer_load_dwordx4 %[vo3], s[44:47], s56 offen offset:2048 nt lds\n"
    "v_fma_f32 v161, v161, s65, v210\n"
    "v_fma_f32 v162, v162, s65, v210\n"
    "v_mfma_f32_16x16x32_bf16 v[188:191], a[124:127], a[60:63], v[188:191]\n"
    "ds_read_b128 a[72:75], %[kb] offset:33280\n"
    "v_fma_f32 v163, v163, s65, v210\n"
    "v_fma_f32 v164, v164, s65, v210\n"
    "v_mfma_f32_16x16x32_bf16 a[228:231], a[240:243], v[144:147], a[228:231]\n"
    "ds_read_b128 a[76:79], %[kb] offset:33536\n"
    "v_fma_f32 v165, v165, s65, v210\n"
    "v_mfma_f32_16x16x32_bf16 a[228:231], a[240:243], v[152:155], a[228:231]\n"
    "ds_read_b128 a[80:83], %[kb] offset:36864\n"
    "v_fma_f32 v166, v166, s65, v210\n"
    "v_mfma_f32_16x16x32_bf16 v[32:35], a[128:131], v[144:147], v[32:35]\n"
    "ds_read_b128 a[84:87], %[kb] offset:37120\n"
    "v_fma_f32 v167, v167, s65, v210\n"
    "v_mfma_f32_16x16x32_bf16 v[32:35], a[132:135], v[152:155], v[32:35]\n"
    "buffer_store_dwordx4 a[216:219], %[ost], s[52:55], s73 offen offset:2048 sc1\n"
    "v_fma_f32 v168, v168, s65, v210\n"
    "v_mfma_f32_16x16x32_bf16 v[36:39], a[136:139], v[144:147], v[36:39]\n"
    "ds_read_b128 a[88:91], %[kb] offset:37376\n"
    "v_fma_f32 v169, v169, s65, v210\n"
    "v_mfma_f32_16x16x32_bf16 v[36:39], a[140:143], v[152:155], v[36:39]\n"
    "ds_read_b128 a[92:95], %[kb] offset:37632\n"
    "v_fma_f32 v170, v170, s65, v210\n"
    "v_mfma_f32_16x16x32_bf16 v[40:43], a[144:147], v[144:147], v[40:43]\n"
    "ds_read_b128 a[96:99], %[kb] offset:40960\n"
    "v_fma_f32 v171, v171, s65, v210\n"
    "v_mfma_f32_16x16x32_bf16 v[40:43], a[148:151], v[152:155], v[40:43]\n"
    "ds_read_b128 a[100:103], %[kb] offset:41216\n"
    "v_fma_f32 v172, v172, s65, v210\n"
    "v_mfma_f32_16x16x32_bf16 v[44:47], a[152:155], v[144:147], v[44:47]\n"
    "ds_read_b128 a[104:107], %[kb] offset:41472\n"
    "v_fma_f32 v173, v173, s65, v210\n"
    "v_mfma_f32_16x16x32_bf16 v[44:47], a[156:159], v[152:155], v[44:47]\n"
    "ds_read_b128 a[108:111], %[kb] offset:41728\n"
    "v_fma_f32 v174, v174, s65, v210\n"
    "v_mfma_f32_16x16x32_bf16 v[48:51], a[160:163], v[144:147], v[48:51]\n"
    "ds_read_b128 a[112:115], %[kb] offset:45056\n"
    "v_fma_f32 v175, v175, s65, v210\n"
    "v_mfma_f32_16x16x32_bf16 v[48:51], a[164:167], v[152:155], v[48:51]\n"
    "ds_read_b128 a[116:119], %[kb] offset:45312\n"
    "v_fma_f32 v176, v176, s65, v211\n"
    "v_mfma_f32_16x16x32_bf16 v[52:55], a[168:171], v[144:147], v[52:55]\n"
    "ds_read_b128 a[120:123], %[kb] offset:45568\n"
    "v_exp_f32_e32 v160, v160\n"
    "v_mfma_f32_16x16x32_bf16 v[52:55], a[172:175], v[152:155], v[52:55]\n"
    "ds_read_b128 a[124:127], %[kb] offset:45824\n"
    "v_fma_f32 v177, v177, s65, v211\n"
    "v_mfma_f32_16x16x32_bf16 v[56:59], a[176:179], v[144:147], v[56:59]\n"
    "ds_read_b128 a[248:251], %[or0] offset:0\n"
    "v_exp_f32_e32 v161, v161\n"
    "v_mfma_f32_16x16x32_bf16 v[56:59], a[180:183], v[152:155], v[56:59]\n"
    "v_exp_f32_e32 v162, v162\n"
    "v_fma_f32 v178, v178, s65, v211\n"
    "v_mfma_f32_16x16x32_bf16 v[60:63], a[184:187], v[144:147], v[60:63]\n"
    "buffer_load_dwordx4 %[vo1], s[44:47], s56 offen offset:3072 nt lds\n"
    "v_exp_f32_e32 v163, v163\n"
    "v_mfma_f32_16x16x32_bf16 v[60:63], a[188:191], v[152:155], v[60:63]\n"
    "s_waitcnt lgkmcnt(9)\n"
    "s_add_u32 s56, s56, 0x4000\n"
    "v_exp_f32_e32 v164, v164\n"
    "v_mfma_f32_16x16x32_bf16 v[128:131], a[64:67], a[0:3], 0\n"
    "v_exp_f32_e32 v165, v165\n"
    "v_mfma_f32_16x16x32_bf16 v[128:131], a[68:71], a[4:7], v[128:131]\n"
    "v_exp_f32_e32 v166, v166\n"
    "v_mfma_f32_16x16x32_bf16 v[128:131], a[72:75], a[8:11], v[128:131]\n"
    "v_exp_f32_e32 v167, v167\n"
    "v_mfma_f32_16x16x32_bf16 v[128:131], a[76:79], a[12:15], v[128:131]\n"
    "v_exp_f32_e32 v168, v168\n"
    "v_mfma_f32_16x16x32_bf16 v[132:135], a[80:83], a[0:3], 0\n"
    "v_exp_f32_e32 v169, v169\n"
    "v_mfma_f32_16x16x32_bf16 v[132:135], a[84:87], a[4:7], v[132:135]\n"
    "v_exp_f32_e32 v170, v170\n"
    "v_mfma_f32_16x16x32_bf16 v[132:135], a[88:91], a[8:11], v[132:135]\n"
    "v_exp_f32_e32 v171, v171\n"
    "v_mfma_f32_16x16x32_bf16 v[132:135], a[92:95], a[12:15], v[132:135]\n"
    "s_waitcnt lgkmcnt(1)\n"
    "v_exp_f32_e32 v172, v172\n"
    "v_mfma_f32_16x16x32_bf16 v[136:139], a[96:99], a[0:3], 0\n"
    "v_exp_f32_e32 v173, v173\n"
    "v_mfma_f32_16x16x32_bf16 v[136:139], a[100:103], a[4:7], v[136:139]\n"
    "v_exp_f32_e32 v174, v174\n"
    "v_mfma_f32_16x16x32_bf16 v[136:139], a[104:107], a[8:11], v[136:139]\n"
    "v_exp_f32_e32 v175, v175\n"
    "v_mfma_f32_16x16x32_bf16 v[136:139], a[108:111], a[12:15], v[136:139]\n"
    "v_cvt_pk_bf16_f32 v160, v160, v161\n"
    "v_cvt_pk_bf16_f32 v161, v162, v163\n"
    "v_mfma_f32_16x16x32_bf16 v[140:143], a[112:115], a[0:3], 0\n"
    "v_cvt_pk_bf16_f32 v162, v164, v165\n"
    "v_cvt_pk_bf16_f32 v163, v166, v167\n"
    "v_mfma_f32_16x16x32_bf16 v[140:143], a[116:119], a[4:7], v[140:143]\n"
    "v_cvt_pk_bf16_f32 v168, v168, v169\n"
    "v_cvt_pk_bf16_f32 v169, v170, v171\n"
    "v_mfma_f32_16x16x32_bf16 v[140:143], a[120:123], a[8:11], v[140:143]\n"
    "v_cvt_pk_bf16_f32 v170, v172, v173\n"
    "v_cvt_pk_bf16_f32 v171, v174, v175\n"
    "v_mfma_f32_16x16x32_bf16 v[140:143], a[124:127], a[12:15], v[140:143]\n"
    "s_add_u32 m0, s60, 0x14000\n"
    "v_fma_f32 v179, v179, s65, v211\n"
    "v_fma_f32 v180, v180, s65, v211\n"
    "v_mfma_f32_16x16x32_bf16 a[232:235], a[240:243], v[160:163], a[232:235]\n"
    "buffer_load_dwordx4 %[vo0], s[48:51], s57 offen nt lds\n"
    "v_fma_f32 v181, v181, s65, v211\n"
    "v_mfma_f32_16x16x32_bf16 a[232:235], a[240:243], v[168:171], a[232:235]\n"
    "v_fma_f32 v182, v182, s65, v211\n"
    "v_fma_f32 v183, v183, s65, v211\n"
    "v_mfma_f32_16x16x32_bf16 v[64:67], a[128:131], v[160:163], v[64:67]\n"
    "buffer_store_dwordx4 a[220:223], %[ost], s[52:55], s73 offen offset:3072 sc1\n"
    "v_fma_f32 v184, v184, s65, v211\n"
    "v_mfma_f32_16x16x32_bf16 v[64:67], a[132:135], v[168:171], v[64:67]\n"
    "v_fma_f32 v185, v185, s65, v211\n"
    "v_fma_f32 v186, v186, s65, v211\n"
    "v_mfma_f32_16x16x32_bf16 v[68:71], a[136:139], v[160:163], v[68:71]\n"
    "v_fma_f32 v187, v187, s65, v211\n"
    "v_fma_f32 v188, v188, s65, v211\n"
    "v_mfma_f32_16x16x32_bf16 v[68:71], a[140:143], v[168:171], v[68:71]\n"
    "v_fma_f32 v189, v189, s65, v211\n"
    "v_fma_f32 v190, v190, s65, v211\n"
    "v_mfma_f32_16x16x32_bf16 v[72:75], a[144:147], v[160:163], v[72:75]\n"
    "ds_read_b128 a[252:255], %[or1] offset:1024\n"
    "v_fma_f32 v191, v191, s65, v211\n"
    "v_mfma_f32_16x16x32_bf16 v[72:75], a[148:151], v[168:171], v[72:75]\n"
    "v_exp_f32_e32 v176, v176\n"
    "v_mfma_f32_16x16x32_bf16 v[76:79], a[152:155], v[160:163], v[76:79]\n"
    "v_exp_f32_e32 v177, v177\n"
    "v_mfma_f32_16x16x32_bf16 v[76:79], a[156:159], v[168:171], v[76:79]\n"
    "v_exp_f32_e32 v178, v178\n"
    "v_mfma_f32_16x16x32_bf16 v[80:83], a[160:163], v[160:163], v[80:83]\n"
    "v_exp_f32_e32 v179, v179\n"
    "v_mfma_f32_16x16x32_bf16 v[80:83], a[164:167], v[168:171], v[80:83]\n"
    "v_exp_f32_e32 v180, v180\n"
    "v_mfma_f32_16x16x32_bf16 v[84:87], a[168:171], v[160:163], v[84:87]\n"
    "v_exp_f32_e32 v181, v181\n"
    "v_mfma_f32_16x16x32_bf16 v[84:87], a[172:175], v[168:171], v[84:87]\n"
    "v_exp_f32_e32 v182, v182\n"
    "v_mfma_f32_16x16x32_bf16 v[88:91], a[176:179], v[160:163], v[88:91]\n"
    "v_exp_f32_e32 v183, v183\n"
    "v_mfma_f32_16x16x32_bf16 v[88:91], a[180:183], v[168:171], v[88:91]\n"
    "v_exp_f32_e32 v184, v184\n"
    "v_mfma_f32_16x16x32_bf16 v[92:95], a[184:187], v[160:163], v[92:95]\n"
    "v_exp_f32_e32 v185, v185\n"
    "v_mfma_f32_16x16x32_bf16 v[92:95], a[188:191], v[168:171], v[92:95]\n"
    "buffer_load_dwordx4 %[vo2], s[48:51], s57 offen offset:1024 nt lds\n"
    "v_cvt_pk_bf16_f32 v176, v176, v177\n"
    "v_mfma_f32_16x16x32_bf16 v[144:147], a[64:67], a[16:19], 0\n"
    "v_exp_f32_e32 v186, v186\n"
    "v_mfma_f32_16x16x32_bf16 v[144:147], a[68:71], a[20:23], v[144:147]\n"
    "v_exp_f32_e32 v187, v187\n"
    "v_mfma_f32_16x16x32_bf16 v[144:147], a[72:75], a[24:27], v[144:147]\n"
    "v_exp_f32_e32 v188, v188\n"
    "v_mfma_f32_16x16x32_bf16 v[144:147], a[76:79], a[28:31], v[144:147]\n"
    "v_exp_f32_e32 v189, v189\n"
    "v_mfma_f32_16x16x32_bf16 v[148:151], a[80:83], a[16:19], 0\n"
    "v_exp_f32_e32 v190, v190\n"
    "v_mfma_f32_16x16x32_bf16 v[148:151], a[84:87], a[20:23], v[148:151]\n"
    "v_exp_f32_e32 v191, v191\n"
    "v_mfma_f32_16x16x32_bf16 v[148:151], a[88:91], a[24:27], v[148:151]\n"
    "v_cvt_pk_bf16_f32 v177, v178, v179\n"
    "v_cvt_pk_bf16_f32 v178, v180, v181\n"
    "v_mfma_f32_16x16x32_bf16 v[148:151], a[92:95], a[28:31], v[148:151]\n"
    "v_cvt_pk_bf16_f32 v179, v182, v183\n"
    "v_cvt_pk_bf16_f32 v184, v184, v185\n"
    "v_mfma_f32_16x16x32_bf16 v[152:155], a[96:99], a[16:19], 0\n"
    "v_cvt_pk_bf16_f32 v185, v186, v187\n"
    "v_cvt_pk_bf16_f32 v186, v188, v189\n"
    "v_mfma_f32_16x16x32_bf16 v[152:155], a[100:103], a[20:23], v[152:155]\n"
    "v_cvt_pk_bf16_f32 v187, v190, v191\n"
    "v_fma_f32 v128, v128, s65, v208\n"
    "v_mfma_f32_16x16x32_bf16 v[152:155], a[104:107], a[24:27], v[152:155]\n"
    "s_add_u32 s73, s59, 0x2000\n"
    "v_fma_f32 v129, v129, s65, v208\n"
    "v_fma_f32 v130, v130, s65, v208\n"
    "v_mfma_f32_16x16x32_bf16 v[152:155], a[108:111], a[28:31], v[152:155]\n"
    "v_fma_f32 v131, v131, s65, v208\n"
    "v_fma_f32 v132, v132, s65, v208\n"
    "v_mfma_f32_16x16x32_bf16 v[156:159], a[112:115], a[16:19], 0\n"
    "v_fma_f32 v133, v133, s65, v208\n"
    "v_fma_f32 v134, v134, s65, v208\n"
    "v_mfma_f32_16x16x32_bf16 v[156:159], a[116:119], a[20:23], v[156:159]\n"
    "v_fma_f32 v135, v135, s65, v208\n"
    "v_fma_f32 v136, v136, s65, v208\n"
    "v_mfma_f32_16x16x32_bf16 v[156:159], a[120:123], a[24:27], v[156:159]\n"
    "v_fma_f32 v137, v137, s65, v208\n"
    "v_fma_f32 v138, v138, s65, v208\n"
    "v_mfma_f32_16x16x32_bf16 v[156:159], a[124:127], a[28:31], v[156:159]\n"
    "v_fma_f32 v139, v139, s65, v208\n"
    "v_fma_f32 v140, v140, s65, v208\n"
    "v_mfma_f32_16x16x32_bf16 a[236:239], a[240:243], v[176:179], a[236:239]\n"
    "v_fma_f32 v141, v141, s65, v208\n"
    "v_fma_f32 v142, v142, s65, v208\n"
    "v_mfma_f32_16x16x32_bf16 a[236:239], a[240:243], v[184:187], a[236:239]\n"
    "buffer_load_dwordx4 %[vo3], s[48:51], s57 offen offset:2048 nt lds\n"
    "v_fma_f32 v143, v143, s65, v208\n"
    "v_mfma_f32_16x16x32_bf16 v[96:99], a[128:131], v[176:179], v[96:99]\n"
    "s_waitcnt lgkmcnt(0)\n"
    "buffer_store_dwordx4 a[248:251], %[ost], s[52:55], s73 offen sc1\n"
    "v_fma_f32 v144, v144, s65, v209\n"
    "v_mfma_f32_16x16x32_bf16 v[96:99], a[132:135], v[184:187], v[96:99]\n"
    "ds_read_b64_tr_b16 a[128:129], %[vb0] offset:32768\n"
    "v_fma_f32 v145, v145, s65, v209\n"
    "v_mfma_f32_16x16x32_bf16 v[100:103], a[136:139], v[176:179], v[100:103]\n"
    "ds_read_b64_tr_b16 a[130:131], %[vb0] offset:36864\n"
    "v_fma_f32 v146, v146, s65, v209\n"
    "v_mfma_f32_16x16x32_bf16 v[100:103], a[140:143], v[184:187], v[100:103]\n"
    "ds_read_b64_tr_b16 a[132:133], %[vb0] offset:40960\n"
    "v_fma_f32 v147, v147, s65, v209\n"
    "v_mfma_f32_16x16x32_bf16 v[104:107], a[144:147], v[176:179], v[104:107]\n"
    "ds_read_b64_tr_b16 a[134:135], %[vb0] offset:45056\n"
    "v_fma_f32 v148, v148, s65, v209\n"
    "v_mfma_f32_16x16x32_bf16 v[104:107], a[148:151], v[184:187], v[104:107]\n"
    "ds_read_b64_tr_b16 a[136:137], %[vb1] offset:32768\n"
    "v_fma_f32 v149, v149, s65, v209\n"
    "v_mfma_f32_16x16x32_bf16 v[108:111], a[152:155], v[176:179], v[108:111]\n"
    "ds_read_b64_tr_b16 a[138:139], %[vb1] offset:36864\n"
    "v_exp_f32_e32 v128, v128\n"
    "v_mfma_f32_16x16x32_bf16 v[108:111], a[156:159], v[184:187], v[108:111]\n"
    "ds_read_b64_tr_b16 a[140:141], %[vb1] offset:40960\n"
    "v_fma_f32 v150, v150, s65, v209\n"
    "v_mfma_f32_16x16x32_bf16 v[112:115], a[160:163], v[176:179], v[112:115]\n"
    "ds_read_b64_tr_b16 a[142:143], %[vb1] offset:45056\n"
    "v_exp_f32_e32 v129, v129\n"
    "v_mfma_f32_16x16x32_bf16 v[112:115], a[164:167], v[184:187], v[112:115]\n"
    "ds_read_b64_tr_b16 a[144:145], %[vb0] offset:33024\n"
    "v_fma_f32 v151, v151, s65, v209\n"
    "v_mfma_f32_16x16x32_bf16 v[116:119], a[168:171], v[176:179], v[116:119]\n"
    "ds_read_b64_tr_b16 a[146:147], %[vb0] offset:37120\n"
    "v_exp_f32_e32 v130, v130\n"
    "v_mfma_f32_16x16x32_bf16 v[116:119], a[172:175], v[184:187], v[116:119]\n"
    "v_exp_f32_e32 v131, v131\n"
    "v_mfma_f32_16x16x32_bf16 v[120:123], a[176:179], v[176:179], v[120:123]\n"
    "v_exp_f32_e32 v132, v132\n"
    "v_mfma_f32_16x16x32_bf16 v[120:123], a[180:183], v[184:187], v[120:123]\n"
    "v_exp_f32_e32 v133, v133\n"
    "v_mfma_f32_16x16x32_bf16 v[124:127], a[184:187], v[176:179], v[124:127]\n"
    "v_exp_f32_e32 v134, v134\n"
    "v_mfma_f32_16x16x32_bf16 v[124:127], a[188:191], v[184:187], v[124:127]\n"
    "v_exp_f32_e32 v135, v135\n"
    "v_mfma_f32_16x16x32_bf16 v[160:163], a[64:67], a[32:35], 0\n"
    "v_exp_f32_e32 v136, v136\n"
    "v_mfma_f32_16x16x32_bf16 v[160:163], a[68:71], a[36:39], v[160:163]\n"
    "buffer_load_dwordx4 %[vo1], s[48:51], s57 offen offset:3072 nt lds\n"
    "v_cvt_pk_bf16_f32 v128, v128, v129\n"
    "v_mfma_f32_16x16x32_bf16 v[160:163], a[72:75], a[40:43], v[160:163]\n"
    "s_add_u32 s57, s57, 0x4000\n"
    "v_exp_f32_e32 v137, v137\n"
    "v_mfma_f32_16x16x32_bf16 v[160:163], a[76:79], a[44:47], v[160:163]\n"
    "v_exp_f32_e32 v138, v138\n"
    "v_mfma_f32_16x16x32_bf16 v[164:167], a[80:83], a[32:35], 0\n"
    "v_exp_f32_e32 v139, v139\n"
    "v_mfma_f32_16x16x32_bf16 v[164:167], a[84:87], a[36:39], v[164:167]\n"
    "v_exp_f32_e32 v140, v140\n"
    "v_mfma_f32_16x16x32_bf16 v[164:167], a[88:91], a[40:43], v[164:167]\n"
    "v_exp_f32_e32 v141, v141\n"
    "v_mfma_f32_16x16x32_bf16 v[164:167], a[92:95], a[44:47], v[164:167]\n"
    "v_exp_f32_e32 v142, v142\n"
    "v_mfma_f32_16x16x32_bf16 v[168:171], a[96:99], a[32:35], 0\n"
    "v_exp_f32_e32 v143, v143\n"
    "v_mfma_f32_16x16x32_bf16 v[168:171], a[100:103], a[36:39], v[168:171]\n"
    "ds_read_b64_tr_b16 a[148:149], %[vb0] offset:41216\n"
    "v_cvt_pk_bf16_f32 v129, v130, v131\n"
    "v_mfma_f32_16x16x32_bf16 v[168:171], a[104:107], a[40:43], v[168:171]\n"
    "ds_read_b64_tr_b16 a[150:151], %[vb0] offset:45312\n"
    "v_cvt_pk_bf16_f32 v130, v132, v133\n"
    "v_mfma_f32_16x16x32_bf16 v[168:171], a[108:111], a[44:47], v[168:171]\n"
    "ds_read_b64_tr_b16 a[152:153], %[vb1] offset:33024\n"
    "v_cvt_pk_bf16_f32 v131, v134, v135\n"
    "v_mfma_f32_16x16x32_bf16 v[172:175], a[112:115], a[32:35], 0\n"
    "ds_read_b64_tr_b16 a[154:155], %[vb1] offset:37120\n"
    "v_cvt_pk_bf16_f32 v136, v136, v137\n"
    "v_cvt_pk_bf16_f32 v137, v138, v139\n"
    "v_mfma_f32_16x16x32_bf16 v[172:175], a[116:119], a[36:39], v[172:175]\n"
    "v_cvt_pk_bf16_f32 v138, v140, v141\n"
    "v_cvt_pk_bf16_f32 v139, v142, v143\n"
    "v_mfma_f32_16x16x32_bf16 v[172:175], a[120:123], a[40:43], v[172:175]\n"
    "ds_read_b64_tr_b16 a[156:157], %[vb1] offset:41216\n"
    "v_fma_f32 v152, v152, s65, v209\n"
    "v_mfma_f32_16x16x32_bf16 v[172:175], a[124:127], a[44:47], v[172:175]\n"
    "ds_read_b64_tr_b16 a[158:159], %[vb1] offset:45312\n"
    "v_fma_f32 v153, v153, s65, v209\n"
    "v_mfma_f32_16x16x32_bf16 a[224:227], a[240:243], v[128:131], a[224:227]\n"
    "ds_read_b64_tr_b16 a[160:161], %[vb0] offset:33280\n"
    "ds_read_b64_tr_b16 a[162:163], %[vb0] offset:37376\n"
    "v_fma_f32 v154, v154, s65, v209\n"
    "v_mfma_f32_16x16x32_bf16 a[224:227], a[240:243], v[136:139], a[224:227]\n"
    "s_waitcnt lgkmcnt(8)\n"
    "ds_read_b64_tr_b16 a[164:165], %[vb0] offset:41472\n"
    "ds_read_b64_tr_b16 a[166:167], %[vb0] offset:45568\n"
    "v_fma_f32 v155, v155, s65, v209\n"
    "v_mfma_f32_16x16x32_bf16 v[0:3], a[128:131], v[128:131], v[0:3]\n"
    "ds_read_b64_tr_b16 a[168:169], %[vb1] offset:33280\n"
    "ds_read_b64_tr_b16 a[170:171], %[vb1] offset:37376\n"
    "v_fma_f32 v156, v156, s65, v209\n"
    "v_mfma_f32_16x16x32_bf16 v[0:3], a[132:135], v[136:139], v[0:3]\n"
    "ds_read_b64_tr_b16 a[172:173], %[vb1] offset:41472\n"
    "ds_read_b64_tr_b16 a[174:175], %[vb1] offset:45568\n"
    "v_fma_f32 v157, v157, s65, v209\n"
    "v_mfma_f32_16x16x32_bf16 v[4:7], a[136:139], v[128:131], v[4:7]\n"
    "ds_read_b64_tr_b16 a[176:177], %[vb0] offset:33536\n"
    "ds_read_b64_tr_b16 a[178:179], %[vb0] offset:37632\n"
    "v_fma_f32 v158, v158, s65, v209\n"
    "v_mfma_f32_16x16x32_bf16 v[4:7], a[140:143], v[136:139], v[4:7]\n"
    "ds_read_b64_tr_b16 a[180:181], %[vb0] offset:41728\n"
    "ds_read_b64_tr_b16 a[182:183], %[vb0] offset:45824\n"
    "v_fma_f32 v159, v159, s65, v209\n"
    "v_mfma_f32_16x16x32_bf16 v[8:11], a[144:147], v[128:131], v[8:11]\n"
    "s_waitcnt lgkmcnt(15)\n"
    "ds_read_b64_tr_b16 a[184:185], %[vb1] offset:33536\n"
    "ds_read_b64_tr_b16 a[186:187], %[vb1] offset:37632\n"
    "v_fma_f32 v160, v160, s65, v210\n"
    "v_mfma_f32_16x16x32_bf16 v[8:11], a[148:151], v[136:139], v[8:11]\n"
    "s_waitcnt lgkmcnt(15)\n"
    "buffer_load_dwordx4 a[192:195], %[vq], s[40:43], s58 offen\n"
    "ds_read_b64_tr_b16 a[188:189], %[vb1] offset:41728\n"
    "ds_read_b64_tr_b16 a[190:191], %[vb1] offset:45824\n"
    "v_mfma_f32_16x16x32_bf16 v[12:15], a[152:155], v[128:131], v[12:15]\n"
    "s_waitcnt lgkmcnt(15)\n"
    "buffer_store_dwordx4 a[252:255], %[ost], s[52:55], s73 offen offset:1024 sc1\n"
    "v_fma_f32 v161, v161, s65, v210\n"
    "v_mfma_f32_16x16x32_bf16 v[12:15], a[156:159], v[136:139], v[12:15]\n"
    "s_waitcnt lgkmcnt(14)\n"
    "ds_read_b128 a[248:251], %[or0] offset:2048\n"
    "v_fma_f32 v162, v162, s65, v210\n"
    "v_mfma_f32_16x16x32_bf16 v[16:19], a[160:163], v[128:131], v[16:19]\n"
    "s_waitcnt lgkmcnt(13)\n"
    "v_exp_f32_e32 v144, v144\n"
    "v_mfma_f32_16x16x32_bf16 v[16:19], a[164:167], v[136:139], v[16:19]\n"
    "s_waitcnt lgkmcnt(11)\n"
    "v_exp_f32_e32 v145, v145\n"
    "v_mfma_f32_16x16x32_bf16 v[20:23], a[168:171], v[128:131], v[20:23]\n"
    "s_waitcnt lgkmcnt(9)\n"
    "v_exp_f32_e32 v146, v146\n"
    "v_mfma_f32_16x16x32_bf16 v[20:23], a[172:175], v[136:139], v[20:23]\n"
    "s_waitcnt lgkmcnt(7)\n"
    "v_exp_f32_e32 v147, v147\n"
    "v_mfma_f32_16x16x32_bf16 v[24:27], a[176:179], v[128:131], v[24:27]\n"
    "s_waitcnt lgkmcnt(5)\n"
    "v_exp_f32_e32 v148, v148\n"
    "v_mfma_f32_16x16x32_bf16 v[24:27], a[180:183], v[136:139], v[24:27]\n"
    "s_waitcnt lgkmcnt(3)\n"
    "v_exp_f32_e32 v149, v149\n"
    "v_mfma_f32_16x16x32_bf16 v[28:31], a[184:187], v[128:131], v[28:31]\n"
    "s_waitcnt lgkmcnt(1)\n"
    "v_exp_f32_e32 v150, v150\n"
    "v_mfma_f32_16x16x32_bf16 v[28:31], a[188:191], v[136:139], v[28:31]\n"
    "v_exp_f32_e32 v151, v151\n"
    "s_waitcnt vmcnt(21)\n"
    ".Lpace3_%=:\n"
    "s_memrealtime s[74:75]\n"
    "s_waitcnt lgkmcnt(0)\n"
    "s_sub_u32 s74, s74, s36\n"
    "s_cmp_lt_i32 s74, s76\n"
    "s_cbranch_scc1 .Lpace3_%=\n"
    "s_barrier\n"
    "v_mfma_f32_16x16x32_bf16 v[176:179], a[64:67], a[48:51], 0\n"
    "s_cmp_eq_u32 s62, 1\n"
    "s_cselect_b32 s77, 0, s77\n"
    "s_cselect_b32 s71, 0, s71\n"
    "v_exp_f32_e32 v152, v152\n"
    "v_mfma_f32_16x16x32_bf16 v[176:179], a[68:71], a[52:55], v[176:179]\n"
    "s_sub_i32 s74, s74, 4\n"
    "s_add_u32 m0, s60, 0x8000\n"
    "s_nop 0\n"
    "buffer_load_dwordx4 %[vo0], s[44:47], s56 offen nt lds\n"
    "v_cvt_pk_bf16_f32 v144, v144, v145\n"
    "v_mfma_f32_16x16x32_bf16 v[176:179], a[72:75], a[56:59], v[176:179]\n"
    "s_max_i32 s76, s74, s38\n"
    "v_exp_f32_e32 v153, v153\n"
    "v_mfma_f32_16x16x32_bf16 v[176:179], a[76:79], a[60:63], v[176:179]\n"
    "s_add_i32 s75, s77, -8\n"
    "v_exp_f32_e32 v154, v154\n"
    "v_mfma_f32_16x16x32_bf16 v[180:183], a[80:83], a[48:51], 0\n"
    "s_add_u32 s76, s76, s75\n"
    "v_exp_f32_e32 v155, v155\n"
    "v_mfma_f32_16x16x32_bf16 v[180:183], a[84:87], a[52:55], v[180:183]\n"
    "s_add_u32 s38, s38, s75\n"
    "v_exp_f32_e32 v156, v156\n"
    "v_mfma_f32_16x16x32_bf16 v[180:183], a[88:91], a[56:59], v[180:183]\n"
    "s_sub_i32 s77, s77, 16\n"
    "v_exp_f32_e32 v157, v157\n"
    "v_mfma_f32_16x16x32_bf16 v[180:183], a[92:95], a[60:63], v[180:183]\n"
    "s_max_i32 s77, s77, s71\n"
    "v_exp_f32_e32 v158, v158\n"
    "v_mfma_f32_16x16x32_bf16 v[184:187], a[96:99], a[48:51], 0\n"
    "v_exp_f32_e32 v159, v159\n"
    "v_mfma_f32_16x16x32_bf16 v[184:187], a[100:103], a[52:55], v[184:187]\n"
    "v_cvt_pk_bf16_f32 v145, v146, v147\n"
    "v_cvt_pk_bf16_f32 v146, v148, v149\n"
    "v_mfma_f32_16x16x32_bf16 v[184:187], a[104:107], a[56:59], v[184:187]\n"
    "v_cvt_pk_bf16_f32 v147, v150, v151\n"
    "v_cvt_pk_bf16_f32 v152, v152, v153\n"
    "v_mfma_f32_16x16x32_bf16 v[184:187], a[108:111], a[60:63], v[184:187]\n"
    "v_cvt_pk_bf16_f32 v153, v154, v155\n"
    "v_cvt_pk_bf16_f32 v154, v156, v157\n"
    "v_mfma_f32_16x16x32_bf16 v[188:191], a[112:115], a[48:51], 0\n"
    "ds_read_b128 a[64:67], %[kb] offset:49152\n"
    "v_cvt_pk_bf16_f32 v155, v158, v159\n"
    "v_mfma_f32_16x16x32_bf16 v[188:191], a[116:119], a[52:55], v[188:191]\n"
    "ds_read_b128 a[68:71], %[kb] offset:49408\n"
    "v_fma_f32 v163, v163, s65, v210\n"
    "v_mfma_f32_16x16x32_bf16 v[188:191], a[120:123], a[56:59], v[188:191]\n"
    "ds_read_b128 a[72:75], %[kb] offset:49664\n"
    "v_fma_f32 v164, v164, s65, v210\n"
    "v_mfma_f32_16x16x32_bf16 v[188:191], a[124:127], a[60:63], v[188:191]\n"
    "ds_read_b128 a[76:79], %[kb] offset:49920\n"
    "v_fma_f32 v165, v165, s65, v210\n"
    "v_mfma_f32_16x16x32_bf16 a[228:231], a[240:243], v[144:147], a[228:231]\n"
    "ds_read_b128 a[80:83], %[kb] offset:53248\n"
    "v_fma_f32 v166, v166, s65, v210\n"
    "v_mfma_f32_16x16x32_bf16 a[228:231], a[240:243], v[152:155], a[228:231]\n"
    "ds_read_b128 a[84:87], %[kb] offset:53504\n"
    "v_fma_f32 v167, v167, s65, v210\n"
    "v_mfma_f32_16x16x32_bf16 v[32:35], a[128:131], v[144:147], v[32:35]\n"
    "buffer_store_dwordx4 a[248:251], %[ost], s[52:55], s73 offen offset:2048 sc1\n"
    "v_fma_f32 v168, v168, s65, v210\n"
    "v_mfma_f32_16x16x32_bf16 v[32:35], a[132:135], v[152:155], v[32:35]\n"
    "buffer_load_dwordx4 a[196:199], %[vq], s[40:43], s58 offen offset:64\n"
    "v_fma_f32 v169, v169, s65, v210\n"
    "v_mfma_f32_16x16x32_bf16 v[36:39], a[136:139], v[144:147], v[36:39]\n"
    "ds_read_b128 a[88:91], %[kb] offset:53760\n"
    "v_fma_f32 v170, v170, s65, v210\n"
    "v_mfma_f32_16x16x32_bf16 v[36:39], a[140:143], v[152:155], v[36:39]\n"
    "ds_read_b128 a[92:95], %[kb] offset:54016\n"
    "v_fma_f32 v171, v171, s65, v210\n"
    "v_mfma_f32_16x16x32_bf16 v[40:43], a[144:147], v[144:147], v[40:43]\n"
    "ds_read_b128 a[96:99], %[kb] offset:57344\n"
    "v_fma_f32 v172, v172, s65, v210\n"
    "v_mfma_f32_16x16x32_bf16 v[40:43], a[148:151], v[152:155], v[40:43]\n"
    "ds_read_b128 a[100:103], %[kb] offset:57600\n"
    "v_fma_f32 v173, v173, s65, v210\n"
    "v_mfma_f32_16x16x32_bf16 v[44:47], a[152:155], v[144:147], v[44:47]\n"
    "ds_read_b128 a[104:107], %[kb] offset:57856\n"
    "v_fma_f32 v174, v174, s65, v210\n"
    "v_mfma_f32_16x16x32_bf16 v[44:47], a[156:159], v[152:155], v[44:47]\n"
    "ds_read_b128 a[108:111], %[kb] offset:58112\n"
    "v_fma_f32 v175, v175, s65, v210\n"
    "v_mfma_f32_16x16x32_bf16 v[48:51], a[160:163], v[144:147], v[48:51]\n"
    "ds_read_b128 a[112:115], %[kb] offset:61440\n"
    "v_fma_f32 v176, v176, s65, v211\n"
    "v_mfma_f32_16x16x32_bf16 v[48:51], a[164:167], v[152:155], v[48:51]\n"
    "ds_read_b128 a[116:119], %[kb] offset:61696\n"
    "v_exp_f32_e32 v160, v160\n"
    "v_mfma_f32_16x16x32_bf16 v[52:55], a[168:171], v[144:147], v[52:55]\n"
    "ds_read_b128 a[120:123], %[kb] offset:61952\n"
    "v_fma_f32 v177, v177, s65, v211\n"
    "v_mfma_f32_16x16x32_bf16 v[52:55], a[172:175], v[152:155], v[52:55]\n"
    "ds_read_b128 a[124:127], %[kb] offset:62208\n"
    "v_exp_f32_e32 v161, v161\n"
    "v_mfma_f32_16x16x32_bf16 v[56:59], a[176:179], v[144:147], v[56:59]\n"
    "ds_read_b128 a[252:255], %[or1] offset:3072\n"
    "v_fma_f32 v178, v178, s65, v211\n"
    "v_mfma_f32_16x16x32_bf16 v[56:59], a[180:183], v[152:155], v[56:59]\n"
    "buffer_load_dwordx4 %[vo2], s[44:47], s56 offen offset:1024 nt lds\n"
    "v_exp_f32_e32 v162, v162\n"
    "v_mfma_f32_16x16x32_bf16 v[60:63], a[184:187], v[144:147], v[60:63]\n"
    "v_exp_f32_e32 v163, v163\n"
    "v_mfma_f32_16x16x32_bf16 v[60:63], a[188:191], v[152:155], v[60:63]\n"
    "s_waitcnt lgkmcnt(9)\n"
    "v_exp_f32_e32 v164, v164\n"
    "v_mfma_f32_16x16x32_bf16 v[128:131], a[64:67], a[0:3], 0\n"
    "v_exp_f32_e32 v165, v165\n"
    "v_mfma_f32_16x16x32_bf16 v[128:131], a[68:71], a[4:7], v[128:131]\n"
    "v_exp_f32_e32 v166, v166\n"
    "v_mfma_f32_16x16x32_bf16 v[128:131], a[72:75], a[8:11], v[128:131]\n"
    "v_exp_f32_e32 v167, v167\n"
    "v_mfma_f32_16x16x32_bf16 v[128:131], a[76:79], a[12:15], v[128:131]\n"
    "v_exp_f32_e32 v168, v168\n"
    "v_mfma_f32_16x16x32_bf16 v[132:135], a[80:83], a[0:3], 0\n"
    "v_exp_f32_e32 v169, v169\n"
    "v_mfma_f32_16x16x32_bf16 v[132:135], a[84:87], a[4:7], v[132:135]\n"
    "v_exp_f32_e32 v170, v170\n"
    "v_mfma_f32_16x16x32_bf16 v[132:135], a[88:91], a[8:11], v[132:135]\n"
    "v_exp_f32_e32 v171, v171\n"
    "v_mfma_f32_16x16x32_bf16 v[132:135], a[92:95], a[12:15], v[132:135]\n"
    "s_waitcnt lgkmcnt(1)\n"
    "v_exp_f32_e32 v172, v172\n"
    "v_mfma_f32_16x16x32_bf16 v[136:139], a[96:99], a[0:3], 0\n"
    "v_exp_f32_e32 v173, v173\n"
    "v_mfma_f32_16x16x32_bf16 v[136:139], a[100:103], a[4:7], v[136:139]\n"
    "v_exp_f32_e32 v174, v174\n"
    "v_mfma_f32_16x16x32_bf16 v[136:139], a[104:107], a[8:11], v[136:139]\n"
    "v_exp_f32_e32 v175, v175\n"
    "v_mfma_f32_16x16x32_bf16 v[136:139], a[108:111], a[12:15], v[136:139]\n"
    "v_cvt_pk_bf16_f32 v160, v160, v161\n"
    "v_cvt_pk_bf16_f32 v161, v162, v163\n"
    "v_mfma_f32_16x16x32_bf16 v[140:143], a[112:115], a[0:3], 0\n"
    "v_cvt_pk_bf16_f32 v162, v164, v165\n"
    "v_cvt_pk_bf16_f32 v163, v166, v167\n"
    "v_mfma_f32_16x16x32_bf16 v[140:143], a[116:119], a[4:7], v[140:143]\n"
    "v_cvt_pk_bf16_f32 v168, v168, v169\n"
    "v_cvt_pk_bf16_f32 v169, v170, v171\n"
    "v_mfma_f32_16x16x32_bf16 v[140:143], a[120:123], a[8:11], v[140:143]\n"
    "v_cvt_pk_bf16_f32 v170, v172, v173\n"
    "v_cvt_pk_bf16_f32 v171, v174, v175\n"
    "v_mfma_f32_16x16x32_bf16 v[140:143], a[124:127], a[12:15], v[140:143]\n"
    "ds_read_b128 a[248:251], %[or0] offset:4096\n"
    "v_fma_f32 v179, v179, s65, v211\n"
    "v_mfma_f32_16x16x32_bf16 a[232:235], a[240:243], v[160:163], a[232:235]\n"
    "v_fma_f32 v180, v180, s65, v211\n"
    "v_fma_f32 v181, v181, s65, v211\n"
    "v_mfma_f32_16x16x32_bf16 a[232:235], a[240:243], v[168:171], a[232:235]\n"
    "s_waitcnt lgkmcnt(1)\n"
    "buffer_store_dwordx4 a[252:255], %[ost], s[52:55], s73 offen offset:3072 sc1\n"
    "v_fma_f32 v182, v182, s65, v211\n"
    "v_mfma_f32_16x16x32_bf16 v[64:67], a[128:131], v[160:163], v[64:67]\n"
    "v_fma_f32 v183, v183, s65, v211\n"
    "v_fma_f32 v184, v184, s65, v211\n"
    "v_mfma_f32_16x16x32_bf16 v[64:67], a[132:135], v[168:171], v[64:67]\n"
    "v_fma_f32 v185, v185, s65, v211\n"
    "v_fma_f32 v186, v186, s65, v211\n"
    "v_mfma_f32_16x16x32_bf16 v[68:71], a[136:139], v[160:163], v[68:71]\n"
    "v_fma_f32 v187, v187, s65, v211\n"
    "v_fma_f32 v188, v188, s65, v211\n"
    "v_mfma_f32_16x16x32_bf16 v[68:71], a[140:143], v[168:171], v[68:71]\n"
    "buffer_load_dwordx4 a[200:203], %[vq], s[40:43], s58 offen offset:128\n"
    "v_fma_f32 v189, v189, s65, v211\n"
    "v_mfma_f32_16x16x32_bf16 v[72:75], a[144:147], v[160:163], v[72:75]\n"
    "v_fma_f32 v190, v190, s65, v211\n"
    "v_fma_f32 v191, v191, s65, v211\n"
    "v_mfma_f32_16x16x32_bf16 v[72:75], a[148:151], v[168:171], v[72:75]\n"
    "v_exp_f32_e32 v176, v176\n"
    "v_mfma_f32_16x16x32_bf16 v[76:79], a[152:155], v[160:163], v[76:79]\n"
    "v_exp_f32_e32 v177, v177\n"
    "v_mfma_f32_16x16x32_bf16 v[76:79], a[156:159], v[168:171], v[76:79]\n"
    "v_exp_f32_e32 v178, v178\n"
    "v_mfma_f32_16x16x32_bf16 v[80:83], a[160:163], v[160:163], v[80:83]\n"
    "v_exp_f32_e32 v179, v179\n"
    "v_mfma_f32_16x16x32_bf16 v[80:83], a[164:167], v[168:171], v[80:83]\n"
    "v_exp_f32_e32 v180, v180\n"
    "v_mfma_f32_16x16x32_bf16 v[84:87], a[168:171], v[160:163], v[84:87]\n"
    "v_exp_f32_e32 v181, v181\n"
    "v_mfma_f32_16x16x32_bf16 v[84:87], a[172:175], v[168:171], v[84:87]\n"
    "v_exp_f32_e32 v182, v182\n"
    "v_mfma_f32_16x16x32_bf16 v[88:91], a[176:179], v[160:163], v[88:91]\n"
    "v_exp_f32_e32 v183, v183\n"
    "v_mfma_f32_16x16x32_bf16 v[88:91], a[180:183], v[168:171], v[88:91]\n"
    "v_exp_f32_e32 v184, v184\n"
    "v_mfma_f32_16x16x32_bf16 v[92:95], a[184:187], v[160:163], v[92:95]\n"
    "v_exp_f32_e32 v185, v185\n"
    "v_mfma_f32_16x16x32_bf16 v[92:95], a[188:191], v[168:171], v[92:95]\n"
    "v_exp_f32_e32 v186, v186\n"
    "v_mfma_f32_16x16x32_bf16 v[144:147], a[64:67], a[16:19], 0\n"
    "v_exp_f32_e32 v187, v187\n"
    "v_mfma_f32_16x16x32_bf16 v[144:147], a[68:71], a[20:23], v[144:147]\n"
    "v_exp_f32_e32 v188, v188\n"
    "v_mfma_f32_16x16x32_bf16 v[144:147], a[72:75], a[24:27], v[144:147]\n"
    "v_exp_f32_e32 v189, v189\n"
    "v_mfma_f32_16x16x32_bf16 v[144:147], a[76:79], a[28:31], v[144:147]\n"
    "v_exp_f32_e32 v190, v190\n"
    "v_mfma_f32_16x16x32_bf16 v[148:151], a[80:83], a[16:19], 0\n"
    "v_exp_f32_e32 v191, v191\n"
    "v_mfma_f32_16x16x32_bf16 v[148:151], a[84:87], a[20:23], v[148:151]\n"
    "v_cvt_pk_bf16_f32 v176, v176, v177\n"
    "v_cvt_pk_bf16_f32 v177, v178, v179\n"
    "v_mfma_f32_16x16x32_bf16 v[148:151], a[88:91], a[24:27], v[148:151]\n"
    "v_cvt_pk_bf16_f32 v178, v180, v181\n"
    "v_cvt_pk_bf16_f32 v179, v182, v183\n"
    "v_mfma_f32_16x16x32_bf16 v[148:151], a[92:95], a[28:31], v[148:151]\n"
    "v_cvt_pk_bf16_f32 v184, v184, v185\n"
    "v_cvt_pk_bf16_f32 v185, v186, v187\n"
    "v_mfma_f32_16x16x32_bf16 v[152:155], a[96:99], a[16:19], 0\n"
    "v_cvt_pk_bf16_f32 v186, v188, v189\n"
    "v_cvt_pk_bf16_f32 v187, v190, v191\n"
    "v_mfma_f32_16x16x32_bf16 v[152:155], a[100:103], a[20:23], v[152:155]\n"
    "s_add_u32 s73, s59, 0x3000\n"
    "buffer_load_dwordx4 %[vo3], s[44:47], s56 offen offset:2048 nt lds\n"
    "v_fma_f32 v128, v128, s65, v208\n"
    "v_mfma_f32_16x16x32_bf16 v[152:155], a[104:107], a[24:27], v[152:155]\n"
    "ds_read_b128 a[252:255], %[or1] offset:5120\n"
    "v_fma_f32 v129, v129, s65, v208\n"
    "v_mfma_f32_16x16x32_bf16 v[152:155], a[108:111], a[28:31], v[152:155]\n"
    "v_fma_f32 v130, v130, s65, v208\n"
    "v_fma_f32 v131, v131, s65, v208\n"
    "v_mfma_f32_16x16x32_bf16 v[156:159], a[112:115], a[16:19], 0\n"
    "v_fma_f32 v132, v132, s65, v208\n"
    "v_fma_f32 v133, v133, s65, v208\n"
    "v_mfma_f32_16x16x32_bf16 v[156:159], a[116:119], a[20:23], v[156:159]\n"
    "v_fma_f32 v134, v134, s65, v208\n"
    "v_fma_f32 v135, v135, s65, v208\n"
    "v_mfma_f32_16x16x32_bf16 v[156:159], a[120:123], a[24:27], v[156:159]\n"
    "v_fma_f32 v136, v136, s65, v208\n"
    "v_fma_f32 v137, v137, s65, v208\n"
    "v_mfma_f32_16x16x32_bf16 v[156:159], a[124:127], a[28:31], v[156:159]\n"
    "v_fma_f32 v138, v138, s65, v208\n"
    "v_fma_f32 v139, v139, s65, v208\n"
    "v_mfma_f32_16x16x32_bf16 a[236:239], a[240:243], v[176:179], a[236:239]\n"
    "v_fma_f32 v140, v140, s65, v208\n"
    "v_fma_f32 v141, v141, s65, v208\n"
    "v_mfma_f32_16x16x32_bf16 a[236:239], a[240:243], v[184:187], a[236:239]\n"
    "s_waitcnt lgkmcnt(1)\n"
    "buffer_store_dwordx4 a[248:251], %[ost], s[52:55], s73 offen sc1\n"
    "v_fma_f32 v142, v142, s65, v208\n"
    "v_mfma_f32_16x16x32_bf16 v[96:99], a[128:131], v[176:179], v[96:99]\n"
    "ds_read_b64_tr_b16 a[128:129], %[vb0] offset:49152\n"
    "v_fma_f32 v143, v143, s65, v208\n"
    "v_mfma_f32_16x16x32_bf16 v[96:99], a[132:135], v[184:187], v[96:99]\n"
    "ds_read_b64_tr_b16 a[130:131], %[vb0] offset:53248\n"
    "v_fma_f32 v144, v144, s65, v209\n"
    "v_mfma_f32_16x16x32_bf16 v[100:103], a[136:139], v[176:179], v[100:103]\n"
    "ds_read_b64_tr_b16 a[132:133], %[vb0] offset:57344\n"
    "v_fma_f32 v145, v145, s65, v209\n"
    "v_mfma_f32_16x16x32_bf16 v[100:103], a[140:143], v[184:187], v[100:103]\n"
    "ds_read_b64_tr_b16 a[134:135], %[vb0] offset:61440\n"
    "v_fma_f32 v146, v146, s65, v209\n"
    "v_mfma_f32_16x16x32_bf16 v[104:107], a[144:147], v[176:179], v[104:107]\n"
    "ds_read_b64_tr_b16 a[136:137], %[vb1] offset:49152\n"
    "v_fma_f32 v147, v147, s65, v209\n"
    "v_mfma_f32_16x16x32_bf16 v[104:107], a[148:151], v[184:187], v[104:107]\n"
    "ds_read_b64_tr_b16 a[138:139], %[vb1] offset:53248\n"
    "v_fma_f32 v148, v148, s65, v209\n"
    "v_mfma_f32_16x16x32_bf16 v[108:111], a[152:155], v[176:179], v[108:111]\n"
    "buffer_load_dwordx4 a[204:207], %[vq], s[40:43], s58 offen offset:192\n"
    "v_exp_f32_e32 v128, v128\n"
    "v_mfma_f32_16x16x32_bf16 v[108:111], a[156:159], v[184:187], v[108:111]\n"
    "s_add_u32 s58, s58, 0x1000\n"
    "ds_read_b64_tr_b16 a[140:141], %[vb1] offset:57344\n"
    "v_fma_f32 v149, v149, s65, v209\n"
    "v_mfma_f32_16x16x32_bf16 v[112:115], a[160:163], v[176:179], v[112:115]\n"
    "ds_read_b64_tr_b16 a[142:143], %[vb1] offset:61440\n"
    "v_exp_f32_e32 v129, v129\n"
    "v_mfma_f32_16x16x32_bf16 v[112:115], a[164:167], v[184:187], v[112:115]\n"
    "ds_read_b64_tr_b16 a[144:145], %[vb0] offset:49408\n"
    "v_fma_f32 v150, v150, s65, v209\n"
    "v_mfma_f32_16x16x32_bf16 v[116:119], a[168:171], v[176:179], v[116:119]\n"
    "ds_read_b64_tr_b16 a[146:147], %[vb0] offset:53504\n"
    "v_exp_f32_e32 v130, v130\n"
    "v_mfma_f32_16x16x32_bf16 v[116:119], a[172:175], v[184:187], v[116:119]\n"
    "v_exp_f32_e32 v131, v131\n"
    "v_mfma_f32_16x16x32_bf16 v[120:123], a[176:179], v[176:179], v[120:123]\n"
    "v_exp_f32_e32 v132, v132\n"
    "v_mfma_f32_16x16x32_bf16 v[120:123], a[180:183], v[184:187], v[120:123]\n"
    "v_exp_f32_e32 v133, v133\n"
    "v_mfma_f32_16x16x32_bf16 v[124:127], a[184:187], v[176:179], v[124:127]\n"
    "v_exp_f32_e32 v134, v134\n"
    "v_mfma_f32_16x16x32_bf16 v[124:127], a[188:191], v[184:187], v[124:127]\n"
    "v_exp_f32_e32 v135, v135\n"
    "v_mfma_f32_16x16x32_bf16 v[160:163], a[64:67], a[32:35], 0\n"
    "global_load_dword v223, v222, s[78:79]\n"
    "v_cvt_pk_bf16_f32 v128, v128, v129\n"
    "v_mfma_f32_16x16x32_bf16 v[160:163], a[68:71], a[36:39], v[160:163]\n"
    "v_exp_f32_e32 v136, v136\n"
    "v_mfma_f32_16x16x32_bf16 v[160:163], a[72:75], a[40:43], v[160:163]\n"
    "v_exp_f32_e32 v137, v137\n"
    "v_mfma_f32_16x16x32_bf16 v[160:163], a[76:79], a[44:47], v[160:163]\n"
    "v_exp_f32_e32 v138, v138\n"
    "v_mfma_f32_16x16x32_bf16 v[164:167], a[80:83], a[32:35], 0\n"
    "v_exp_f32_e32 v139, v139\n"
    "v_mfma_f32_16x16x32_bf16 v[164:167], a[84:87], a[36:39], v[164:167]\n"
    "v_exp_f32_e32 v140, v140\n"
    "v_mfma_f32_16x16x32_bf16 v[164:167], a[88:91], a[40:43], v[164:167]\n"
    "v_exp_f32_e32 v141, v141\n"
    "v_mfma_f32_16x16x32_bf16 v[164:167], a[92:95], a[44:47], v[164:167]\n"
    "v_exp_f32_e32 v142, v142\n"
    "v_mfma_f32_16x16x32_bf16 v[168:171], a[96:99], a[32:35], 0\n"
    "v_exp_f32_e32 v143, v143\n"
    "v_mfma_f32_16x16x32_bf16 v[168:171], a[100:103], a[36:39], v[168:171]\n"
    "ds_read_b64_tr_b16 a[148:149], %[vb0] offset:57600\n"
    "v_cvt_pk_bf16_f32 v129, v130, v131\n"
    "v_mfma_f32_16x16x32_bf16 v[168:171], a[104:107], a[40:43], v[168:171]\n"
    "ds_read_b64_tr_b16 a[150:151], %[vb0] offset:61696\n"
    "v_cvt_pk_bf16_f32 v130, v132, v133\n"
    "v_mfma_f32_16x16x32_bf16 v[168:171], a[108:111], a[44:47], v[168:171]\n"
    "ds_read_b64_tr_b16 a[152:153], %[vb1] offset:49408\n"
    "v_cvt_pk_bf16_f32 v131, v134, v135\n"
    "v_mfma_f32_16x16x32_bf16 v[172:175], a[112:115], a[32:35], 0\n"
    "ds_read_b64_tr_b16 a[154:155], %[vb1] offset:53504\n"
    "v_cvt_pk_bf16_f32 v136, v136, v137\n"
    "v_cvt_pk_bf16_f32 v137, v138, v139\n"
    "v_mfma_f32_16x16x32_bf16 v[172:175], a[116:119], a[36:39], v[172:175]\n"
    "ds_read_b64_tr_b16 a[156:157], %[vb1] offset:57600\n"
    "v_cvt_pk_bf16_f32 v138, v140, v141\n"
    "v_cvt_pk_bf16_f32 v139, v142, v143\n"
    "v_mfma_f32_16x16x32_bf16 v[172:175], a[120:123], a[40:43], v[172:175]\n"
    "ds_read_b64_tr_b16 a[158:159], %[vb1] offset:61696\n"
    "v_fma_f32 v151, v151, s65, v209\n"
    "v_mfma_f32_16x16x32_bf16 v[172:175], a[124:127], a[44:47], v[172:175]\n"
    "ds_read_b64_tr_b16 a[160:161], %[vb0] offset:49664\n"
    "v_fma_f32 v152, v152, s65, v209\n"
    "v_mfma_f32_16x16x32_bf16 a[224:227], a[240:243], v[128:131], a[224:227]\n"
    "ds_read_b64_tr_b16 a[162:163], %[vb0] offset:53760\n"
    "v_fma_f32 v153, v153, s65, v209\n"
    "v_mfma_f32_16x16x32_bf16 a[224:227], a[240:243], v[136:139], a[224:227]\n"
    "s_waitcnt lgkmcnt(8)\n"
    "ds_read_b64_tr_b16 a[164:165], %[vb0] offset:57856\n"
    "ds_read_b64_tr_b16 a[166:167], %[vb0] offset:61952\n"
    "v_fma_f32 v154, v154, s65, v209\n"
    "v_mfma_f32_16x16x32_bf16 v[0:3], a[128:131], v[128:131], v[0:3]\n"
    "ds_read_b64_tr_b16 a[168:169], %[vb1] offset:49664\n"
    "ds_read_b64_tr_b16 a[170:171], %[vb1] offset:53760\n"
    "v_fma_f32 v155, v155, s65, v209\n"
    "v_mfma_f32_16x16x32_bf16 v[0:3], a[132:135], v[136:139], v[0:3]\n"
    "ds_read_b64_tr_b16 a[172:173], %[vb1] offset:57856\n"
    "ds_read_b64_tr_b16 a[174:175], %[vb1] offset:61952\n"
    "v_fma_f32 v156, v156, s65, v209\n"
    "v_mfma_f32_16x16x32_bf16 v[4:7], a[136:139], v[128:131], v[4:7]\n"
    "ds_read_b64_tr_b16 a[176:177], %[vb0] offset:49920\n"
    "ds_read_b64_tr_b16 a[178:179], %[vb0] offset:54016\n"
    "v_fma_f32 v157, v157, s65, v209\n"
    "v_mfma_f32_16x16x32_bf16 v[4:7], a[140:143], v[136:139], v[4:7]\n"
    "ds_read_b64_tr_b16 a[180:181], %[vb0] offset:58112\n"
    "ds_read_b64_tr_b16 a[182:183], %[vb0] offset:62208\n"
    "v_fma_f32 v158, v158, s65, v209\n"
    "v_mfma_f32_16x16x32_bf16 v[8:11], a[144:147], v[128:131], v[8:11]\n"
    "s_waitcnt lgkmcnt(15)\n"
    "ds_read_b64_tr_b16 a[184:185], %[vb1] offset:49920\n"
    "ds_read_b64_tr_b16 a[186:187], %[vb1] offset:54016\n"
    "v_fma_f32 v159, v159, s65, v209\n"
    "v_mfma_f32_16x16x32_bf16 v[8:11], a[148:151], v[136:139], v[8:11]\n"
    "s_waitcnt lgkmcnt(15)\n"
    "buffer_store_dwordx4 a[252:255], %[ost], s[52:55], s73 offen offset:1024 sc1\n"
    "ds_read_b64_tr_b16 a[188:189], %[vb1] offset:58112\n"
    "ds_read_b64_tr_b16 a[190:191], %[vb1] offset:62208\n"
    "v_mfma_f32_16x16x32_bf16 v[12:15], a[152:155], v[128:131], v[12:15]\n"
    "s_waitcnt lgkmcnt(15)\n"
    "buffer_load_dwordx4 %[vo1], s[44:47], s56 offen offset:3072 nt lds\n"
    "v_fma_f32 v160, v160, s65, v210\n"
    "v_mfma_f32_16x16x32_bf16 v[12:15], a[156:159], v[136:139], v[12:15]\n"
    "s_waitcnt lgkmcnt(14)\n"
    "s_add_u32 s56, s56, 0x4000\n"
    "ds_read_b128 a[248:251], %[or0] offset:6144\n"
    "v_fma_f32 v161, v161, s65, v210\n"
    "v_mfma_f32_16x16x32_bf16 v[16:19], a[160:163], v[128:131], v[16:19]\n"
    "s_waitcnt lgkmcnt(13)\n"
    "buffer_load_dwordx4 a[208:211], %[vq], s[40:43], s58 offen\n"
    "v_fma_f32 v162, v162, s65, v210\n"
    "v_mfma_f32_16x16x32_bf16 v[16:19], a[164:167], v[136:139], v[16:19]\n"
    "s_waitcnt lgkmcnt(11)\n"
    "v_exp_f32_e32 v144, v144\n"
    "v_mfma_f32_16x16x32_bf16 v[20:23], a[168:171], v[128:131], v[20:23]\n"
    "s_waitcnt lgkmcnt(9)\n"
    "v_exp_f32_e32 v145, v145\n"
    "v_mfma_f32_16x16x32_bf16 v[20:23], a[172:175], v[136:139], v[20:23]\n"
    "s_waitcnt lgkmcnt(7)\n"
    "v_exp_f32_e32 v146, v146\n"
    "v_mfma_f32_16x16x32_bf16 v[24:27], a[176:179], v[128:131], v[24:27]\n"
    "s_waitcnt lgkmcnt(5)\n"
    "v_exp_f32_e32 v147, v147\n"
    "v_mfma_f32_16x16x32_bf16 v[24:27], a[180:183], v[136:139], v[24:27]\n"
    "s_waitcnt lgkmcnt(3)\n"
    "v_exp_f32_e32 v148, v148\n"
    "v_mfma_f32_16x16x32_bf16 v[28:31], a[184:187], v[128:131], v[28:31]\n"
    "s_waitcnt lgkmcnt(1)\n"
    "v_exp_f32_e32 v149, v149\n"
    "v_mfma_f32_16x16x32_bf16 v[28:31], a[188:191], v[136:139], v[28:31]\n"
    "v_exp_f32_e32 v150, v150\n"
    "s_waitcnt vmcnt(26)\n"
    ".Lpace4_%=:\n"
    "s_memrealtime s[74:75]\n"
    "s_waitcnt lgkmcnt(0)\n"
    "s_sub_u32 s74, s74, s36\n"
    "s_cmp_lt_i32 s74, s76\n"
    "s_cbranch_scc1 .Lpace4_%=\n"
    "s_barrier\n"
    "v_mfma_f32_16x16x32_bf16 v[176:179], a[64:67], a[48:51], 0\n"
    "s_sub_i32 s74, s74, 4\n"
    "v_exp_f32_e32 v151, v151\n"
    "v_mfma_f32_16x16x32_bf16 v[176:179], a[68:71], a[52:55], v[176:179]\n"
    "s_max_i32 s76, s74, s38\n"
    "v_exp_f32_e32 v152, v152\n"
    "v_mfma_f32_16x16x32_bf16 v[176:179], a[72:75], a[56:59], v[176:179]\n"
    "s_add_i32 s75, s77, -14\n"
    "v_exp_f32_e32 v153, v153\n"
    "v_mfma_f32_16x16x32_bf16 v[176:179], a[76:79], a[60:63], v[176:179]\n"
    "s_add_u32 s76, s76, s75\n"
    "v_exp_f32_e32 v154, v154\n"
    "v_mfma_f32_16x16x32_bf16 v[180:183], a[80:83], a[48:51], 0\n"
    "s_add_u32 s38, s38, s75\n"
    "v_exp_f32_e32 v155, v155\n"
    "v_mfma_f32_16x16x32_bf16 v[180:183], a[84:87], a[52:55], v[180:183]\n"
    "s_sub_i32 s77, s77, 16\n"
    "v_exp_f32_e32 v156, v156\n"
    "v_mfma_f32_16x16x32_bf16 v[180:183], a[88:91], a[56:59], v[180:183]\n"
    "s_max_i32 s77, s77, s71\n"
    "v_exp_f32_e32 v157, v157\n"
    "v_mfma_f32_16x16x32_bf16 v[180:183], a[92:95], a[60:63], v[180:183]\n"
    "v_exp_f32_e32 v158, v158\n"
    "v_mfma_f32_16x16x32_bf16 v[184:187], a[96:99], a[48:51], 0\n"
    "v_exp_f32_e32 v159, v159\n"
    "v_mfma_f32_16x16x32_bf16 v[184:187], a[100:103], a[52:55], v[184:187]\n"
    "v_cvt_pk_bf16_f32 v144, v144, v145\n"
    "v_cvt_pk_bf16_f32 v145, v146, v147\n"
    "v_mfma_f32_16x16x32_bf16 v[184:187], a[104:107], a[56:59], v[184:187]\n"
    "v_cvt_pk_bf16_f32 v146, v148, v149\n"
    "v_cvt_pk_bf16_f32 v147, v150, v151\n"
    "v_mfma_f32_16x16x32_bf16 v[184:187], a[108:111], a[60:63], v[184:187]\n"
    "v_cvt_pk_bf16_f32 v152, v152, v153\n"
    "v_cvt_pk_bf16_f32 v153, v154, v155\n"
    "v_mfma_f32_16x16x32_bf16 v[188:191], a[112:115], a[48:51], 0\n"
    "ds_read_b128 a[64:67], %[kb] offset:0\n"
    "v_cvt_pk_bf16_f32 v154, v156, v157\n"
    "v_cvt_pk_bf16_f32 v155, v158, v159\n"
    "v_mfma_f32_16x16x32_bf16 v[188:191], a[116:119], a[52:55], v[188:191]\n"
    "ds_read_b128 a[68:71], %[kb] offset:256\n"
    "v_fma_f32 v163, v163, s65, v210\n"
    "v_mfma_f32_16x16x32_bf16 v[188:191], a[120:123], a[56:59], v[188:191]\n"
    "ds_read_b128 a[72:75], %[kb] offset:512\n"
    "v_fma_f32 v164, v164, s65, v210\n"
    "v_mfma_f32_16x16x32_bf16 v[188:191], a[124:127], a[60:63], v[188:191]\n"
    "ds_read_b128 a[76:79], %[kb] offset:768\n"
    "v_fma_f32 v165, v165, s65, v210\n"
    "v_mfma_f32_16x16x32_bf16 a[228:231], a[240:243], v[144:147], a[228:231]\n"
    "ds_read_b128 a[80:83], %[kb] offset:4096\n"
    "v_fma_f32 v166, v166, s65, v210\n"
    "v_mfma_f32_16x16x32_bf16 a[228:231], a[240:243], v[152:155], a[228:231]\n"
    "buffer_store_dwordx4 a[248:251], %[ost], s[52:55], s73 offen offset:2048 sc1\n"
    "v_fma_f32 v167, v167, s65, v210\n"
    "v_mfma_f32_16x16x32_bf16 v[32:35], a[128:131], v[144:147], v[32:35]\n"
    "ds_read_b128 a[84:87], %[kb] offset:4352\n"
    "v_fma_f32 v168, v168, s65, v210\n"
    "v_mfma_f32_16x16x32_bf16 v[32:35], a[132:135], v[152:155], v[32:35]\n"
    "ds_read_b128 a[88:91], %[kb] offset:4608\n"
    "v_fma_f32 v169, v169, s65, v210\n"
    "v_mfma_f32_16x16x32_bf16 v[36:39], a[136:139], v[144:147], v[36:39]\n"
    "ds_read_b128 a[92:95], %[kb] offset:4864\n"
    "v_fma_f32 v170, v170, s65, v210\n"
    "v_mfma_f32_16x16x32_bf16 v[36:39], a[140:143], v[152:155], v[36:39]\n"
    "ds_read_b128 a[96:99], %[kb] offset:8192\n"
    "v_fma_f32 v171, v171, s65, v210\n"
    "v_mfma_f32_16x16x32_bf16 v[40:43], a[144:147], v[144:147], v[40:43]\n"
    "ds_read_b128 a[100:103], %[kb] offset:8448\n"
    "v_fma_f32 v172, v172, s65, v210\n"
    "v_mfma_f32_16x16x32_bf16 v[40:43], a[148:151], v[152:155], v[40:43]\n"
    "ds_read_b128 a[104:107], %[kb] offset:8704\n"
    "v_fma_f32 v173, v173, s65, v210\n"
    "v_mfma_f32_16x16x32_bf16 v[44:47], a[152:155], v[144:147], v[44:47]\n"
    "ds_read_b128 a[108:111], %[kb] offset:8960\n"
    "v_fma_f32 v174, v174, s65, v210\n"
    "v_mfma_f32_16x16x32_bf16 v[44:47], a[156:159], v[152:155], v[44:47]\n"
    "ds_read_b128 a[112:115], %[kb] offset:12288\n"
    "v_fma_f32 v175, v175, s65, v210\n"
    "v_mfma_f32_16x16x32_bf16 v[48:51], a[160:163], v[144:147], v[48:51]\n"
    "ds_read_b128 a[116:119], %[kb] offset:12544\n"
    "v_fma_f32 v176, v176, s65, v211\n"
    "v_mfma_f32_16x16x32_bf16 v[48:51], a[164:167], v[152:155], v[48:51]\n"
    "ds_read_b128 a[120:123], %[kb] offset:12800\n"
    "v_exp_f32_e32 v160, v160\n"
    "v_mfma_f32_16x16x32_bf16 v[52:55], a[168:171], v[144:147], v[52:55]\n"
    "ds_read_b128 a[124:127], %[kb] offset:13056\n"
    "v_fma_f32 v177, v177, s65, v211\n"
    "v_mfma_f32_16x16x32_bf16 v[52:55], a[172:175], v[152:155], v[52:55]\n"
    "buffer_load_dwordx4 a[212:215], %[vq], s[40:43], s58 offen offset:64\n"
    "v_exp_f32_e32 v161, v161\n"
    "v_mfma_f32_16x16x32_bf16 v[56:59], a[176:179], v[144:147], v[56:59]\n"
    "ds_read_b128 a[252:255], %[or1] offset:7168\n"
    "v_fma_f32 v178, v178, s65, v211\n"
    "v_mfma_f32_16x16x32_bf16 v[56:59], a[180:183], v[152:155], v[56:59]\n"
    "v_exp_f32_e32 v162, v162\n"
    "v_cvt_pk_bf16_f32 v160, v160, v161\n"
    "v_mfma_f32_16x16x32_bf16 v[60:63], a[184:187], v[144:147], v[60:63]\n"
    "v_exp_f32_e32 v163, v163\n"
    "v_mfma_f32_16x16x32_bf16 v[60:63], a[188:191], v[152:155], v[60:63]\n"
    "s_waitcnt lgkmcnt(8)\n"
    "v_exp_f32_e32 v164, v164\n"
    "v_mfma_f32_16x16x32_bf16 v[128:131], a[64:67], a[0:3], 0\n"
    "v_exp_f32_e32 v165, v165\n"
    "v_mfma_f32_16x16x32_bf16 v[128:131], a[68:71], a[4:7], v[128:131]\n"
    "v_exp_f32_e32 v166, v166\n"
    "v_mfma_f32_16x16x32_bf16 v[128:131], a[72:75], a[8:11], v[128:131]\n"
    "v_exp_f32_e32 v167, v167\n"
    "v_mfma_f32_16x16x32_bf16 v[128:131], a[76:79], a[12:15], v[128:131]\n"
    "v_exp_f32_e32 v168, v168\n"
    "v_mfma_f32_16x16x32_bf16 v[132:135], a[80:83], a[0:3], 0\n"
    "v_exp_f32_e32 v169, v169\n"
    "v_mfma_f32_16x16x32_bf16 v[132:135], a[84:87], a[4:7], v[132:135]\n"
    "s_add_u32 m0, s60, 0x18000\n"
    "v_exp_f32_e32 v170, v170\n"
    "v_mfma_f32_16x16x32_bf16 v[132:135], a[88:91], a[8:11], v[132:135]\n"
    "buffer_load_dwordx4 %[vo0], s[48:51], s57 offen nt lds\n"
    "v_cvt_pk_bf16_f32 v161, v162, v163\n"
    "v_mfma_f32_16x16x32_bf16 v[132:135], a[92:95], a[12:15], v[132:135]\n"
    "v_exp_f32_e32 v171, v171\n"
    "v_mfma_f32_16x16x32_bf16 v[136:139], a[96:99], a[0:3], 0\n"
    "s_waitcnt lgkmcnt(0)\n"
    "v_exp_f32_e32 v172, v172\n"
    "v_mfma_f32_16x16x32_bf16 v[136:139], a[100:103], a[4:7], v[136:139]\n"
    "v_exp_f32_e32 v173, v173\n"
    "v_mfma_f32_16x16x32_bf16 v[136:139], a[104:107], a[8:11], v[136:139]\n"
    "v_exp_f32_e32 v174, v174\n"
    "v_mfma_f32_16x16x32_bf16 v[136:139], a[108:111], a[12:15], v[136:139]\n"
    "v_exp_f32_e32 v175, v175\n"
    "v_mfma_f32_16x16x32_bf16 v[140:143], a[112:115], a[0:3], 0\n"
    "v_cvt_pk_bf16_f32 v162, v164, v165\n"
    "v_cvt_pk_bf16_f32 v163, v166, v167\n"
    "v_mfma_f32_16x16x32_bf16 v[140:143], a[116:119], a[4:7], v[140:143]\n"
    "v_cvt_pk_bf16_f32 v168, v168, v169\n"
    "v_cvt_pk_bf16_f32 v169, v170, v171\n"
    "v_mfma_f32_16x16x32_bf16 v[140:143], a[120:123], a[8:11], v[140:143]\n"
    "v_cvt_pk_bf16_f32 v170, v172, v173\n"
    "v_cvt_pk_bf16_f32 v171, v174, v175\n"
    "v_mfma_f32_16x16x32_bf16 v[140:143], a[124:127], a[12:15], v[140:143]\n"
    "v_fma_f32 v179, v179, s65, v211\n"
    "v_fma_f32 v180, v180, s65, v211\n"
    "v_mfma_f32_16x16x32_bf16 a[232:235], a[240:243], v[160:163], a[232:235]\n"
    "v_fma_f32 v181, v181, s65, v211\n"
    "v_fma_f32 v182, v182, s65, v211\n"
    "v_mfma_f32_16x16x32_bf16 a[232:235], a[240:243], v[168:171], a[232:235]\n"
    "buffer_store_dwordx4 a[252:255], %[ost], s[52:55], s73 offen offset:3072 sc1\n"
    "v_fma_f32 v183, v183, s65, v211\n"
    "v_mfma_f32_16x16x32_bf16 v[64:67], a[128:131], v[160:163], v[64:67]\n"
    "v_fma_f32 v184, v184, s65, v211\n"
    "v_fma_f32 v185, v185, s65, v211\n"
    "v_mfma_f32_16x16x32_bf16 v[64:67], a[132:135], v[168:171], v[64:67]\n"
    "v_fma_f32 v186, v186, s65, v211\n"
    "v_fma_f32 v187, v187, s65, v211\n"
    "v_mfma_f32_16x16x32_bf16 v[68:71], a[136:139], v[160:163], v[68:71]\n"
    "v_fma_f32 v188, v188, s65, v211\n"
    "v_fma_f32 v189, v189, s65, v211\n"
    "v_mfma_f32_16x16x32_bf16 v[68:71], a[140:143], v[168:171], v[68:71]\n"
    "v_fma_f32 v190, v190, s65, v211\n"
    "v_fma_f32 v191, v191, s65, v211\n"
    "v_mfma_f32_16x16x32_bf16 v[72:75], a[144:147], v[160:163], v[72:75]\n"
    "v_exp_f32_e32 v176, v176\n"
    "v_mfma_f32_16x16x32_bf16 v[72:75], a[148:151], v[168:171], v[72:75]\n"
    "s_add_u32 s59, s59, s69\n"
    "v_exp_f32_e32 v177, v177\n"
    "v_mfma_f32_16x16x32_bf16 v[76:79], a[152:155], v[160:163], v[76:79]\n"
    "s_mov_b32 s69, 0x10000\n"
    "v_exp_f32_e32 v178, v178\n"
    "v_mfma_f32_16x16x32_bf16 v[76:79], a[156:159], v[168:171], v[76:79]\n"
    "s_mov_b32 s54, 0x40000\n"
    "v_exp_f32_e32 v179, v179\n"
    "v_mfma_f32_16x16x32_bf16 v[80:83], a[160:163], v[160:163], v[80:83]\n"
    "v_exp_f32_e32 v180, v180\n"
    "v_mfma_f32_16x16x32_bf16 v[80:83], a[164:167], v[168:171], v[80:83]\n"
    "v_exp_f32_e32 v181, v181\n"
    "v_mfma_f32_16x16x32_bf16 v[84:87], a[168:171], v[160:163], v[84:87]\n"
    "v_exp_f32_e32 v182, v182\n"
    "v_mfma_f32_16x16x32_bf16 v[84:87], a[172:175], v[168:171], v[84:87]\n"
    "v_exp_f32_e32 v183, v183\n"
    "v_mfma_f32_16x16x32_bf16 v[88:91], a[176:179], v[160:163], v[88:91]\n"
    "v_exp_f32_e32 v184, v184\n"
    "v_mfma_f32_16x16x32_bf16 v[88:91], a[180:183], v[168:171], v[88:91]\n"
    "buffer_load_dwordx4 a[216:219], %[vq], s[40:43], s58 offen offset:128\n"
    "v_cvt_pk_bf16_f32 v176, v176, v177\n"
    "v_mfma_f32_16x16x32_bf16 v[92:95], a[184:187], v[160:163], v[92:95]\n"
    "v_exp_f32_e32 v185, v185\n"
    "v_mfma_f32_16x16x32_bf16 v[92:95], a[188:191], v[168:171], v[92:95]\n"
    "buffer_load_dwordx4 %[vo2], s[48:51], s57 offen offset:1024 nt lds\n"
    "v_cvt_pk_bf16_f32 v177, v178, v179\n"
    "v_mfma_f32_16x16x32_bf16 v[144:147], a[64:67], a[16:19], 0\n"
    "v_exp_f32_e32 v186, v186\n"
    "v_mfma_f32_16x16x32_bf16 v[144:147], a[68:71], a[20:23], v[144:147]\n"
    "v_exp_f32_e32 v187, v187\n"
    "v_mfma_f32_16x16x32_bf16 v[144:147], a[72:75], a[24:27], v[144:147]\n"
    "v_exp_f32_e32 v188, v188\n"
    "v_mfma_f32_16x16x32_bf16 v[144:147], a[76:79], a[28:31], v[144:147]\n"
    "v_exp_f32_e32 v189, v189\n"
    "v_mfma_f32_16x16x32_bf16 v[148:151], a[80:83], a[16:19], 0\n"
    "v_exp_f32_e32 v190, v190\n"
    "v_mfma_f32_16x16x32_bf16 v[148:151], a[84:87], a[20:23], v[148:151]\n"
    "v_exp_f32_e32 v191, v191\n"
    "v_mfma_f32_16x16x32_bf16 v[148:151], a[88:91], a[24:27], v[148:151]\n"
    "v_cvt_pk_bf16_f32 v178, v180, v181\n"
    "v_cvt_pk_bf16_f32 v179, v182, v183\n"
    "v_mfma_f32_16x16x32_bf16 v[148:151], a[92:95], a[28:31], v[148:151]\n"
    "v_cvt_pk_bf16_f32 v184, v184, v185\n"
    "v_cvt_pk_bf16_f32 v185, v186, v187\n"
    "v_mfma_f32_16x16x32_bf16 v[152:155], a[96:99], a[16:19], 0\n"
    "v_cvt_pk_bf16_f32 v186, v188, v189\n"
    "v_cvt_pk_bf16_f32 v187, v190, v191\n"
    "v_mfma_f32_16x16x32_bf16 v[152:155], a[100:103], a[20:23], v[152:155]\n"
    "v_fma_f32 v128, v128, s65, v208\n"
    "v_fma_f32 v129, v129, s65, v208\n"
    "v_mfma_f32_16x16x32_bf16 v[152:155], a[104:107], a[24:27], v[152:155]\n"
    "v_fma_f32 v130, v130, s65, v208\n"
    "v_fma_f32 v131, v131, s65, v208\n"
    "v_mfma_f32_16x16x32_bf16 v[152:155], a[108:111], a[28:31], v[152:155]\n"
    "v_fma_f32 v132, v132, s65, v208\n"
    "v_fma_f32 v133, v133, s65, v208\n"
    "v_mfma_f32_16x16x32_bf16 v[156:159], a[112:115], a[16:19], 0\n"
    "v_fma_f32 v134, v134, s65, v208\n"
    "v_fma_f32 v135, v135, s65, v208\n"
    "v_mfma_f32_16x16x32_bf16 v[156:159], a[116:119], a[20:23], v[156:159]\n"
    "v_fma_f32 v136, v136, s65, v208\n"
    "v_fma_f32 v137, v137, s65, v208\n"
    "v_mfma_f32_16x16x32_bf16 v[156:159], a[120:123], a[24:27], v[156:159]\n"
    "v_fma_f32 v138, v138, s65, v208\n"
    "v_fma_f32 v139, v139, s65, v208\n"
    "v_mfma_f32_16x16x32_bf16 v[156:159], a[124:127], a[28:31], v[156:159]\n"
    "v_fma_f32 v140, v140, s65, v208\n"
    "v_fma_f32 v141, v141, s65, v208\n"
    "v_mfma_f32_16x16x32_bf16 a[236:239], a[240:243], v[176:179], a[236:239]\n"
    "v_fma_f32 v142, v142, s65, v208\n"
    "v_fma_f32 v143, v143, s65, v208\n"
    "v_mfma_f32_16x16x32_bf16 a[236:239], a[240:243], v[184:187], a[236:239]\n"
    "v_exp_f32_e32 v128, v128\n"
    "v_mfma_f32_16x16x32_bf16 v[96:99], a[128:131], v[176:179], v[96:99]\n"
    "buffer_load_dwordx4 %[vo3], s[48:51], s57 offen offset:2048 nt lds\n"
    "v_fma_f32 v144, v144, s65, v209\n"
    "v_mfma_f32_16x16x32_bf16 v[96:99], a[132:135], v[184:187], v[96:99]\n"
    "ds_read_b64_tr_b16 a[128:129], %[vb0] offset:0\n"
    "v_fma_f32 v145, v145, s65, v209\n"
    "v_mfma_f32_16x16x32_bf16 v[100:103], a[136:139], v[176:179], v[100:103]\n"
    "ds_read_b64_tr_b16 a[130:131], %[vb0] offset:4096\n"
    "v_fma_f32 v146, v146, s65, v209\n"
    "v_mfma_f32_16x16x32_bf16 v[100:103], a[140:143], v[184:187], v[100:103]\n"
    "ds_read_b64_tr_b16 a[132:133], %[vb0] offset:8192\n"
    "v_fma_f32 v147, v147, s65, v209\n"
    "v_mfma_f32_16x16x32_bf16 v[104:107], a[144:147], v[176:179], v[104:107]\n"
    "ds_read_b64_tr_b16 a[134:135], %[vb0] offset:12288\n"
    "v_fma_f32 v148, v148, s65, v209\n"
    "v_mfma_f32_16x16x32_bf16 v[104:107], a[148:151], v[184:187], v[104:107]\n"
    "ds_read_b64_tr_b16 a[136:137], %[vb1] offset:0\n"
    "v_fma_f32 v149, v149, s65, v209\n"
    "v_mfma_f32_16x16x32_bf16 v[108:111], a[152:155], v[176:179], v[108:111]\n"
    "ds_read_b64_tr_b16 a[138:139], %[vb1] offset:4096\n"
    "v_fma_f32 v150, v150, s65, v209\n"
    "v_mfma_f32_16x16x32_bf16 v[108:111], a[156:159], v[184:187], v[108:111]\n"
    "ds_read_b64_tr_b16 a[140:141], %[vb1] offset:8192\n"
    "v_exp_f32_e32 v129, v129\n"
    "v_mfma_f32_16x16x32_bf16 v[112:115], a[160:163], v[176:179], v[112:115]\n"
    "ds_read_b64_tr_b16 a[142:143], %[vb1] offset:12288\n"
    "v_fma_f32 v151, v151, s65, v209\n"
    "v_mfma_f32_16x16x32_bf16 v[112:115], a[164:167], v[184:187], v[112:115]\n"
    "ds_read_b64_tr_b16 a[144:145], %[vb0] offset:256\n"
    "v_exp_f32_e32 v130, v130\n"
    "v_mfma_f32_16x16x32_bf16 v[116:119], a[168:171], v[176:179], v[116:119]\n"
    "ds_read_b64_tr_b16 a[146:147], %[vb0] offset:4352\n"
    "v_cvt_pk_bf16_f32 v128, v128, v129\n"
    "v_mfma_f32_16x16x32_bf16 v[116:119], a[172:175], v[184:187], v[116:119]\n"
    "v_exp_f32_e32 v131, v131\n"
    "v_mfma_f32_16x16x32_bf16 v[120:123], a[176:179], v[176:179], v[120:123]\n"
    "v_exp_f32_e32 v132, v132\n"
    "v_mfma_f32_16x16x32_bf16 v[120:123], a[180:183], v[184:187], v[120:123]\n"
    "v_exp_f32_e32 v133, v133\n"
    "v_mfma_f32_16x16x32_bf16 v[124:127], a[184:187], v[176:179], v[124:127]\n"
    "v_exp_f32_e32 v134, v134\n"
    "v_mfma_f32_16x16x32_bf16 v[124:127], a[188:191], v[184:187], v[124:127]\n"
    "v_exp_f32_e32 v135, v135\n"
    "v_mfma_f32_16x16x32_bf16 v[160:163], a[64:67], a[32:35], 0\n"
    "buffer_load_dwordx4 a[220:223], %[vq], s[40:43], s58 offen offset:192\n"
    "v_cvt_pk_bf16_f32 v129, v130, v131\n"
    "v_mfma_f32_16x16x32_bf16 v[160:163], a[68:71], a[36:39], v[160:163]\n"
    "s_add_u32 s58, s58, 0xf000\n"
    "v_exp_f32_e32 v136, v136\n"
    "v_mfma_f32_16x16x32_bf16 v[160:163], a[72:75], a[40:43], v[160:163]\n"
    "buffer_load_dwordx4 %[vo1], s[48:51], s57 offen offset:3072 nt lds\n"
    "v_cvt_pk_bf16_f32 v130, v132, v133\n"
    "v_mfma_f32_16x16x32_bf16 v[160:163], a[76:79], a[44:47], v[160:163]\n"
    "s_add_u32 s57, s57, 0x4000\n"
    "v_exp_f32_e32 v137, v137\n"
    "v_mfma_f32_16x16x32_bf16 v[164:167], a[80:83], a[32:35], 0\n"
    "v_exp_f32_e32 v138, v138\n"
    "v_mfma_f32_16x16x32_bf16 v[164:167], a[84:87], a[36:39], v[164:167]\n"
    "v_exp_f32_e32 v139, v139\n"
    "v_mfma_f32_16x16x32_bf16 v[164:167], a[88:91], a[40:43], v[164:167]\n"
    "v_exp_f32_e32 v140, v140\n"
    "v_mfma_f32_16x16x32_bf16 v[164:167], a[92:95], a[44:47], v[164:167]\n"
    "v_exp_f32_e32 v141, v141\n"
    "v_mfma_f32_16x16x32_bf16 v[168:171], a[96:99], a[32:35], 0\n"
    "v_exp_f32_e32 v142, v142\n"
    "v_mfma_f32_16x16x32_bf16 v[168:171], a[100:103], a[36:39], v[168:171]\n"
    "v_exp_f32_e32 v143, v143\n"
    "v_mfma_f32_16x16x32_bf16 v[168:171], a[104:107], a[40:43], v[168:171]\n"
    "ds_read_b64_tr_b16 a[148:149], %[vb0] offset:8448\n"
    "v_cvt_pk_bf16_f32 v131, v134, v135\n"
    "v_mfma_f32_16x16x32_bf16 v[168:171], a[108:111], a[44:47], v[168:171]\n"
    "ds_read_b64_tr_b16 a[150:151], %[vb0] offset:12544\n"
    "v_cvt_pk_bf16_f32 v136, v136, v137\n"
    "v_mfma_f32_16x16x32_bf16 v[172:175], a[112:115], a[32:35], 0\n"
    "ds_read_b64_tr_b16 a[152:153], %[vb1] offset:256\n"
    "v_cvt_pk_bf16_f32 v137, v138, v139\n"
    "v_mfma_f32_16x16x32_bf16 v[172:175], a[116:119], a[36:39], v[172:175]\n"
    "ds_read_b64_tr_b16 a[154:155], %[vb1] offset:4352\n"
    "v_cvt_pk_bf16_f32 v138, v140, v141\n"
    "v_mfma_f32_16x16x32_bf16 v[172:175], a[120:123], a[40:43], v[172:175]\n"
    "ds_read_b64_tr_b16 a[156:157], %[vb1] offset:8448\n"
    "v_cvt_pk_bf16_f32 v139, v142, v143\n"
    "v_mfma_f32_16x16x32_bf16 v[172:175], a[124:127], a[44:47], v[172:175]\n"
    "ds_read_b64_tr_b16 a[158:159], %[vb1] offset:12544\n"
    "v_fma_f32 v152, v152, s65, v209\n"
    "v_mfma_f32_16x16x32_bf16 a[224:227], a[240:243], v[128:131], a[224:227]\n"
    "ds_read_b64_tr_b16 a[160:161], %[vb0] offset:512\n"
    "ds_read_b64_tr_b16 a[162:163], %[vb0] offset:4608\n"
    "v_fma_f32 v153, v153, s65, v209\n"
    "v_mfma_f32_16x16x32_bf16 a[224:227], a[240:243], v[136:139], a[224:227]\n"
    "s_waitcnt lgkmcnt(8)\n"
    "ds_read_b64_tr_b16 a[164:165], %[vb0] offset:8704\n"
    "ds_read_b64_tr_b16 a[166:167], %[vb0] offset:12800\n"
    "v_fma_f32 v154, v154, s65, v209\n"
    "v_mfma_f32_16x16x32_bf16 v[0:3], a[128:131], v[128:131], v[0:3]\n"
    "ds_read_b64_tr_b16 a[168:169], %[vb1] offset:512\n"
    "ds_read_b64_tr_b16 a[170:171], %[vb1] offset:4608\n"
    "v_fma_f32 v155, v155, s65, v209\n"
    "v_mfma_f32_16x16x32_bf16 v[0:3], a[132:135], v[136:139], v[0:3]\n"
    "ds_read_b64_tr_b16 a[172:173], %[vb1] offset:8704\n"
    "ds_read_b64_tr_b16 a[174:175], %[vb1] offset:12800\n"
    "v_fma_f32 v156, v156, s65, v209\n"
    "v_mfma_f32_16x16x32_bf16 v[4:7], a[136:139], v[128:131], v[4:7]\n"
    "s_add_u32 m0, s60, 0xc000\n"
    "ds_read_b64_tr_b16 a[176:177], %[vb0] offset:768\n"
    "ds_read_b64_tr_b16 a[178:179], %[vb0] offset:4864\n"
    "v_fma_f32 v157, v157, s65, v209\n"
    "v_mfma_f32_16x16x32_bf16 v[4:7], a[140:143], v[136:139], v[4:7]\n"
    "buffer_load_dwordx4 %[vo0], s[44:47], s56 offen nt lds\n"
    "ds_read_b64_tr_b16 a[180:181], %[vb0] offset:8960\n"
    "ds_read_b64_tr_b16 a[182:183], %[vb0] offset:13056\n"
    "v_mfma_f32_16x16x32_bf16 v[8:11], a[144:147], v[128:131], v[8:11]\n"
    "s_waitcnt lgkmcnt(15)\n"
    "ds_read_b64_tr_b16 a[184:185], %[vb1] offset:768\n"
    "ds_read_b64_tr_b16 a[186:187], %[vb1] offset:4864\n"
    "v_mfma_f32_16x16x32_bf16 v[8:11], a[148:151], v[136:139], v[8:11]\n"
    "s_waitcnt lgkmcnt(15)\n"
    "ds_read_b64_tr_b16 a[188:189], %[vb1] offset:8960\n"
    "ds_read_b64_tr_b16 a[190:191], %[vb1] offset:13056\n"
    "v_mfma_f32_16x16x32_bf16 v[12:15], a[152:155], v[128:131], v[12:15]\n"
    "s_waitcnt lgkmcnt(15)\n"
    "v_fma_f32 v158, v158, s65, v209\n"
    "v_fma_f32 v159, v159, s65, v209\n"
    "v_mfma_f32_16x16x32_bf16 v[12:15], a[156:159], v[136:139], v[12:15]\n"
    "s_waitcnt lgkmcnt(14)\n"
    "v_exp_f32_e32 v144, v144\n"
    "v_mfma_f32_16x16x32_bf16 v[16:19], a[160:163], v[128:131], v[16:19]\n"
    "s_waitcnt lgkmcnt(12)\n"
    "v_exp_f32_e32 v145, v145\n"
    "v_mfma_f32_16x16x32_bf16 v[16:19], a[164:167], v[136:139], v[16:19]\n"
    "s_waitcnt lgkmcnt(10)\n"
    "buffer_load_dwordx4 %[vo2], s[44:47], s56 offen offset:1024 nt lds\n"
    "v_fma_f32 v160, v160, s65, v210\n"
    "v_mfma_f32_16x16x32_bf16 v[20:23], a[168:171], v[128:131], v[20:23]\n"
    "s_waitcnt lgkmcnt(8)\n"
    "s_add_u32 m0, s61, 0x0\n"
    "v_exp_f32_e32 v146, v146\n"
    "v_mfma_f32_16x16x32_bf16 v[20:23], a[172:175], v[136:139], v[20:23]\n"
    "s_waitcnt lgkmcnt(6)\n"
    "buffer_load_dwordx4 %[vo0], s[40:43], s72 offen nt lds\n"
    "v_cvt_pk_bf16_f32 v144, v144, v145\n"
    "v_mfma_f32_16x16x32_bf16 v[24:27], a[176:179], v[128:131], v[24:27]\n"
    "s_waitcnt lgkmcnt(4)\n"
    "v_exp_f32_e32 v147, v147\n"
    "v_mfma_f32_16x16x32_bf16 v[24:27], a[180:183], v[136:139], v[24:27]\n"
    "s_waitcnt lgkmcnt(2)\n"
    "v_exp_f32_e32 v148, v148\n"
    "v_mfma_f32_16x16x32_bf16 v[28:31], a[184:187], v[128:131], v[28:31]\n"
    "s_waitcnt lgkmcnt(0)\n"
    "v_exp_f32_e32 v149, v149\n"
    "v_mfma_f32_16x16x32_bf16 v[28:31], a[188:191], v[136:139], v[28:31]\n"
    "v_exp_f32_e32 v150, v150\n"
    "s_waitcnt vmcnt(27)\n"
    ".Lpace5_%=:\n"
    "s_memrealtime s[74:75]\n"
    "s_waitcnt lgkmcnt(0)\n"
    "s_sub_u32 s74, s74, s36\n"
    "s_cmp_lt_i32 s74, s76\n"
    "s_cbranch_scc1 .Lpace5_%=\n"
    "s_barrier\n"
    "v_mfma_f32_16x16x32_bf16 v[176:179], a[64:67], a[48:51], 0\n"
    "s_sub_i32 s74, s74, 4\n"
    "v_exp_f32_e32 v151, v151\n"
    "v_mfma_f32_16x16x32_bf16 v[176:179], a[68:71], a[52:55], v[176:179]\n"
    "s_max_i32 s76, s74, s38\n"
    "v_exp_f32_e32 v152, v152\n"
    "v_mfma_f32_16x16x32_bf16 v[176:179], a[72:75], a[56:59], v[176:179]\n"
    "s_add_i32 s75, s77, -16\n"
    "v_exp_f32_e32 v153, v153\n"
    "v_mfma_f32_16x16x32_bf16 v[176:179], a[76:79], a[60:63], v[176:179]\n"
    "s_add_u32 s76, s76, s75\n"
    "v_exp_f32_e32 v154, v154\n"
    "v_mfma_f32_16x16x32_bf16 v[180:183], a[80:83], a[48:51], 0\n"
    "s_add_u32 s38, s38, s75\n"
    "v_exp_f32_e32 v155, v155\n"
    "v_mfma_f32_16x16x32_bf16 v[180:183], a[84:87], a[52:55], v[180:183]\n"
    "s_sub_i32 s77, s77, 16\n"
    "v_exp_f32_e32 v156, v156\n"
    "v_mfma_f32_16x16x32_bf16 v[180:183], a[88:91], a[56:59], v[180:183]\n"
    "s_max_i32 s77, s77, s71\n"
    "v_exp_f32_e32 v157, v157\n"
    "v_mfma_f32_16x16x32_bf16 v[180:183], a[92:95], a[60:63], v[180:183]\n"
    "v_exp_f32_e32 v158, v158\n"
    "v_mfma_f32_16x16x32_bf16 v[184:187], a[96:99], a[48:51], 0\n"
    "v_exp_f32_e32 v159, v159\n"
    "v_mfma_f32_16x16x32_bf16 v[184:187], a[100:103], a[52:55], v[184:187]\n"
    "v_cvt_pk_bf16_f32 v145, v146, v147\n"
    "v_cvt_pk_bf16_f32 v146, v148, v149\n"
    "v_mfma_f32_16x16x32_bf16 v[184:187], a[104:107], a[56:59], v[184:187]\n"
    "v_cvt_pk_bf16_f32 v147, v150, v151\n"
    "v_cvt_pk_bf16_f32 v152, v152, v153\n"
    "v_mfma_f32_16x16x32_bf16 v[184:187], a[108:111], a[60:63], v[184:187]\n"
    "v_cvt_pk_bf16_f32 v153, v154, v155\n"
    "v_cvt_pk_bf16_f32 v154, v156, v157\n"
    "v_mfma_f32_16x16x32_bf16 v[188:191], a[112:115], a[48:51], 0\n"
    "ds_read_b128 a[64:67], %[kb] offset:16384\n"
    "v_cvt_pk_bf16_f32 v155, v158, v159\n"
    "v_fma_f32 v161, v161, s65, v210\n"
    "v_mfma_f32_16x16x32_bf16 v[188:191], a[116:119], a[52:55], v[188:191]\n"
    "s_add_u32 m0, s60, 0xc000\n"
    "ds_read_b128 a[68:71], %[kb] offset:16640\n"
    "v_fma_f32 v162, v162, s65, v210\n"
    "v_mfma_f32_16x16x32_bf16 v[188:191], a[120:123], a[56:59], v[188:191]\n"
    "buffer_load_dwordx4 %[vo3], s[44:47], s56 offen offset:2048 nt lds\n"
    "v_fma_f32 v163, v163, s65, v210\n"
    "v_mfma_f32_16x16x32_bf16 v[188:191], a[124:127], a[60:63], v[188:191]\n"
    "ds_read_b128 a[72:75], %[kb] offset:16896\n"
    "v_fma_f32 v164, v164, s65, v210\n"
    "v_mfma_f32_16x16x32_bf16 a[228:231], a[240:243], v[144:147], a[228:231]\n"
    "ds_read_b128 a[76:79], %[kb] offset:17152\n"
    "v_fma_f32 v165, v165, s65, v210\n"
    "v_mfma_f32_16x16x32_bf16 a[228:231], a[240:243], v[152:155], a[228:231]\n"
    "ds_read_b128 a[80:83], %[kb] offset:20480\n"
    "v_fma_f32 v166, v166, s65, v210\n"
    "v_mfma_f32_16x16x32_bf16 v[32:35], a[128:131], v[144:147], v[32:35]\n"
    "ds_read_b128 a[84:87], %[kb] offset:20736\n"
    "v_fma_f32 v167, v167, s65, v210\n"
    "v_mfma_f32_16x16x32_bf16 v[32:35], a[132:135], v[152:155], v[32:35]\n"
    "ds_read_b128 a[88:91], %[kb] offset:20992\n"
    "v_fma_f32 v168, v168, s65, v210\n"
    "v_mfma_f32_16x16x32_bf16 v[36:39], a[136:139], v[144:147], v[36:39]\n"
    "ds_read_b128 a[92:95], %[kb] offset:21248\n"
    "v_fma_f32 v169, v169, s65, v210\n"
    "v_mfma_f32_16x16x32_bf16 v[36:39], a[140:143], v[152:155], v[36:39]\n"
    "ds_read_b128 a[96:99], %[kb] offset:24576\n"
    "v_fma_f32 v170, v170, s65, v210\n"
    "v_mfma_f32_16x16x32_bf16 v[40:43], a[144:147], v[144:147], v[40:43]\n"
    "ds_read_b128 a[100:103], %[kb] offset:24832\n"
    "v_fma_f32 v171, v171, s65, v210\n"
    "v_mfma_f32_16x16x32_bf16 v[40:43], a[148:151], v[152:155], v[40:43]\n"
    "ds_read_b128 a[104:107], %[kb] offset:25088\n"
    "v_fma_f32 v172, v172, s65, v210\n"
    "v_mfma_f32_16x16x32_bf16 v[44:47], a[152:155], v[144:147], v[44:47]\n"
    "ds_read_b128 a[108:111], %[kb] offset:25344\n"
    "v_fma_f32 v173, v173, s65, v210\n"
    "v_mfma_f32_16x16x32_bf16 v[44:47], a[156:159], v[152:155], v[44:47]\n"
    "ds_read_b128 a[112:115], %[kb] offset:28672\n"
    "v_fma_f32 v174, v174, s65, v210\n"
    "v_mfma_f32_16x16x32_bf16 v[48:51], a[160:163], v[144:147], v[48:51]\n"
    "s_add_u32 m0, s61, 0x0\n"
    "ds_read_b128 a[116:119], %[kb] offset:28928\n"
    "v_fma_f32 v175, v175, s65, v210\n"
    "v_mfma_f32_16x16x32_bf16 v[48:51], a[164:167], v[152:155], v[48:51]\n"
    "buffer_load_dwordx4 %[vo2], s[40:43], s72 offen offset:1024 nt lds\n"
    "v_fma_f32 v176, v176, s65, v211\n"
    "v_mfma_f32_16x16x32_bf16 v[52:55], a[168:171], v[144:147], v[52:55]\n"
    "ds_read_b128 a[120:123], %[kb] offset:29184\n"
    "v_exp_f32_e32 v160, v160\n"
    "v_mfma_f32_16x16x32_bf16 v[52:55], a[172:175], v[152:155], v[52:55]\n"
    "ds_read_b128 a[124:127], %[kb] offset:29440\n"
    "v_fma_f32 v177, v177, s65, v211\n"
    "v_mfma_f32_16x16x32_bf16 v[56:59], a[176:179], v[144:147], v[56:59]\n"
    "v_exp_f32_e32 v161, v161\n"
    "v_fma_f32 v178, v178, s65, v211\n"
    "v_mfma_f32_16x16x32_bf16 v[56:59], a[180:183], v[152:155], v[56:59]\n"
    "v_exp_f32_e32 v162, v162\n"
    "v_fma_f32 v179, v179, s65, v211\n"
    "v_mfma_f32_16x16x32_bf16 v[60:63], a[184:187], v[144:147], v[60:63]\n"
    "v_exp_f32_e32 v163, v163\n"
    "v_cvt_pk_bf16_f32 v160, v160, v161\n"
    "v_mfma_f32_16x16x32_bf16 v[60:63], a[188:191], v[152:155], v[60:63]\n"
    "s_waitcnt lgkmcnt(7)\n"
    "v_exp_f32_e32 v164, v164\n"
    "v_mfma_f32_16x16x32_bf16 v[128:131], a[64:67], a[0:3], 0\n"
    "v_exp_f32_e32 v165, v165\n"
    "v_mfma_f32_16x16x32_bf16 v[128:131], a[68:71], a[4:7], v[128:131]\n"
    "s_add_u32 m0, s60, 0xc000\n"
    "v_exp_f32_e32 v166, v166\n"
    "v_mfma_f32_16x16x32_bf16 v[128:131], a[72:75], a[8:11], v[128:131]\n"
    "buffer_load_dwordx4 %[vo1], s[44:47], s56 offen offset:3072 nt lds\n"
    "v_cvt_pk_bf16_f32 v161, v162, v163\n"
    "v_mfma_f32_16x16x32_bf16 v[128:131], a[76:79], a[12:15], v[128:131]\n"
    "s_add_u32 s56, s56, 0x4000\n"
    "v_exp_f32_e32 v167, v167\n"
    "v_mfma_f32_16x16x32_bf16 v[132:135], a[80:83], a[0:3], 0\n"
    "v_exp_f32_e32 v168, v168\n"
    "v_mfma_f32_16x16x32_bf16 v[132:135], a[84:87], a[4:7], v[132:135]\n"
    "v_exp_f32_e32 v169, v169\n"
    "v_mfma_f32_16x16x32_bf16 v[132:135], a[88:91], a[8:11], v[132:135]\n"
    "v_exp_f32_e32 v170, v170\n"
    "v_mfma_f32_16x16x32_bf16 v[132:135], a[92:95], a[12:15], v[132:135]\n"
    "v_exp_f32_e32 v171, v171\n"
    "v_mfma_f32_16x16x32_bf16 v[136:139], a[96:99], a[0:3], 0\n"
    "s_waitcnt lgkmcnt(0)\n"
    "v_exp_f32_e32 v172, v172\n"
    "v_mfma_f32_16x16x32_bf16 v[136:139], a[100:103], a[4:7], v[136:139]\n"
    "v_exp_f32_e32 v173, v173\n"
    "v_mfma_f32_16x16x32_bf16 v[136:139], a[104:107], a[8:11], v[136:139]\n"
    "v_exp_f32_e32 v174, v174\n"
    "v_mfma_f32_16x16x32_bf16 v[136:139], a[108:111], a[12:15], v[136:139]\n"
    "v_exp_f32_e32 v175, v175\n"
    "v_mfma_f32_16x16x32_bf16 v[140:143], a[112:115], a[0:3], 0\n"
    "v_cvt_pk_bf16_f32 v162, v164, v165\n"
    "v_cvt_pk_bf16_f32 v163, v166, v167\n"
    "v_mfma_f32_16x16x32_bf16 v[140:143], a[116:119], a[4:7], v[140:143]\n"
    "v_cvt_pk_bf16_f32 v168, v168, v169\n"
    "v_cvt_pk_bf16_f32 v169, v170, v171\n"
    "v_mfma_f32_16x16x32_bf16 v[140:143], a[120:123], a[8:11], v[140:143]\n"
    "v_cvt_pk_bf16_f32 v170, v172, v173\n"
    "v_cvt_pk_bf16_f32 v171, v174, v175\n"
    "v_mfma_f32_16x16x32_bf16 v[140:143], a[124:127], a[12:15], v[140:143]\n"
    "v_fma_f32 v180, v180, s65, v211\n"
    "v_fma_f32 v181, v181, s65, v211\n"
    "v_mfma_f32_16x16x32_bf16 a[232:235], a[240:243], v[160:163], a[232:235]\n"
    "v_fma_f32 v182, v182, s65, v211\n"
    "v_fma_f32 v183, v183, s65, v211\n"
    "v_mfma_f32_16x16x32_bf16 a[232:235], a[240:243], v[168:171], a[232:235]\n"
    "v_fma_f32 v184, v184, s65, v211\n"
    "v_fma_f32 v185, v185, s65, v211\n"
    "v_mfma_f32_16x16x32_bf16 v[64:67], a[128:131], v[160:163], v[64:67]\n"
    "v_fma_f32 v186, v186, s65, v211\n"
    "v_fma_f32 v187, v187, s65, v211\n"
    "v_mfma_f32_16x16x32_bf16 v[64:67], a[132:135], v[168:171], v[64:67]\n"
    "v_fma_f32 v188, v188, s65, v211\n"
    "v_fma_f32 v189, v189, s65, v211\n"
    "v_mfma_f32_16x16x32_bf16 v[68:71], a[136:139], v[160:163], v[68:71]\n"
    "v_fma_f32 v190, v190, s65, v211\n"
    "v_fma_f32 v191, v191, s65, v211\n"
    "v_mfma_f32_16x16x32_bf16 v[68:71], a[140:143], v[168:171], v[68:71]\n"
    "v_exp_f32_e32 v176, v176\n"
    "v_mfma_f32_16x16x32_bf16 v[72:75], a[144:147], v[160:163], v[72:75]\n"
    "s_add_u32 m0, s60, 0x1c000\n"
    "v_exp_f32_e32 v177, v177\n"
    "v_mfma_f32_16x16x32_bf16 v[72:75], a[148:151], v[168:171], v[72:75]\n"
    "buffer_load_dwordx4 %[vo0], s[48:51], s57 offen nt lds\n"
    "v_fma_f32 v128, v128, s65, v208\n"
    "v_mfma_f32_16x16x32_bf16 v[76:79], a[152:155], v[160:163], v[76:79]\n"
    "v_exp_f32_e32 v178, v178\n"
    "v_mfma_f32_16x16x32_bf16 v[76:79], a[156:159], v[168:171], v[76:79]\n"
    "v_exp_f32_e32 v179, v179\n"
    "v_mfma_f32_16x16x32_bf16 v[80:83], a[160:163], v[160:163], v[80:83]\n"
    "s_add_u32 m0, s61, 0x0\n"
    "v_exp_f32_e32 v180, v180\n"
    "v_mfma_f32_16x16x32_bf16 v[80:83], a[164:167], v[168:171], v[80:83]\n"
    "buffer_load_dwordx4 %[vo3], s[40:43], s72 offen offset:2048 nt lds\n"
    "v_cvt_pk_bf16_f32 v176, v176, v177\n"
    "v_mfma_f32_16x16x32_bf16 v[84:87], a[168:171], v[160:163], v[84:87]\n"
    "v_exp_f32_e32 v181, v181\n"
    "v_mfma_f32_16x16x32_bf16 v[84:87], a[172:175], v[168:171], v[84:87]\n"
    "v_exp_f32_e32 v182, v182\n"
    "v_mfma_f32_16x16x32_bf16 v[88:91], a[176:179], v[160:163], v[88:91]\n"
    "v_exp_f32_e32 v183, v183\n"
    "v_mfma_f32_16x16x32_bf16 v[88:91], a[180:183], v[168:171], v[88:91]\n"
    "v_exp_f32_e32 v184, v184\n"
    "v_mfma_f32_16x16x32_bf16 v[92:95], a[184:187], v[160:163], v[92:95]\n"
    "v_exp_f32_e32 v185, v185\n"
    "v_mfma_f32_16x16x32_bf16 v[92:95], a[188:191], v[168:171], v[92:95]\n"
    "v_exp_f32_e32 v186, v186\n"
    "v_mfma_f32_16x16x32_bf16 v[144:147], a[64:67], a[16:19], 0\n"
    "v_exp_f32_e32 v187, v187\n"
    "v_mfma_f32_16x16x32_bf16 v[144:147], a[68:71], a[20:23], v[144:147]\n"
    "v_exp_f32_e32 v188, v188\n"
    "v_mfma_f32_16x16x32_bf16 v[144:147], a[72:75], a[24:27], v[144:147]\n"
    "v_exp_f32_e32 v189, v189\n"
    "v_mfma_f32_16x16x32_bf16 v[144:147], a[76:79], a[28:31], v[144:147]\n"
    "v_exp_f32_e32 v190, v190\n"
    "v_mfma_f32_16x16x32_bf16 v[148:151], a[80:83], a[16:19], 0\n"
    "v_exp_f32_e32 v191, v191\n"
    "v_mfma_f32_16x16x32_bf16 v[148:151], a[84:87], a[20:23], v[148:151]\n"
    "v_cvt_pk_bf16_f32 v177, v178, v179\n"
    "v_cvt_pk_bf16_f32 v178, v180, v181\n"
    "v_mfma_f32_16x16x32_bf16 v[148:151], a[88:91], a[24:27], v[148:151]\n"
    "v_cvt_pk_bf16_f32 v179, v182, v183\n"
    "v_cvt_pk_bf16_f32 v184, v184, v185\n"
    "v_mfma_f32_16x16x32_bf16 v[148:151], a[92:95], a[28:31], v[148:151]\n"
    "v_cvt_pk_bf16_f32 v185, v186, v187\n"
    "v_cvt_pk_bf16_f32 v186, v188, v189\n"
    "v_mfma_f32_16x16x32_bf16 v[152:155], a[96:99], a[16:19], 0\n"
    "v_cvt_pk_bf16_f32 v187, v190, v191\n"
    "v_fma_f32 v129, v129, s65, v208\n"
    "v_mfma_f32_16x16x32_bf16 v[152:155], a[100:103], a[20:23], v[152:155]\n"
    "s_add_u32 m0, s60, 0x1c000\n"
    "v_fma_f32 v130, v130, s65, v208\n"
    "v_fma_f32 v131, v131, s65, v208\n"
    "v_mfma_f32_16x16x32_bf16 v[152:155], a[104:107], a[24:27], v[152:155]\n"
    "buffer_load_dwordx4 %[vo2], s[48:51], s57 offen offset:1024 nt lds\n"
    "v_fma_f32 v132, v132, s65, v208\n"
    "v_mfma_f32_16x16x32_bf16 v[152:155], a[108:111], a[28:31], v[152:155]\n"
    "v_fma_f32 v133, v133, s65, v208\n"
    "v_fma_f32 v134, v134, s65, v208\n"
    "v_mfma_f32_16x16x32_bf16 v[156:159], a[112:115], a[16:19], 0\n"
    "v_fma_f32 v135, v135, s65, v208\n"
    "v_fma_f32 v136, v136, s65, v208\n"
    "v_mfma_f32_16x16x32_bf16 v[156:159], a[116:119], a[20:23], v[156:159]\n"
    "v_fma_f32 v137, v137, s65, v208\n"
    "v_fma_f32 v138, v138, s65, v208\n"
    "v_mfma_f32_16x16x32_bf16 v[156:159], a[120:123], a[24:27], v[156:159]\n"
    "v_fma_f32 v139, v139, s65, v208\n"
    "v_fma_f32 v140, v140, s65, v208\n"
    "v_mfma_f32_16x16x32_bf16 v[156:159], a[124:127], a[28:31], v[156:159]\n"
    "v_fma_f32 v141, v141, s65, v208\n"
    "v_fma_f32 v142, v142, s65, v208\n"
    "v_mfma_f32_16x16x32_bf16 a[236:239], a[240:243], v[176:179], a[236:239]\n"
    "v_fma_f32 v143, v143, s65, v208\n"
    "v_fma_f32 v144, v144, s65, v209\n"
    "v_mfma_f32_16x16x32_bf16 a[236:239], a[240:243], v[184:187], a[236:239]\n"
    "v_exp_f32_e32 v128, v128\n"
    "v_mfma_f32_16x16x32_bf16 v[96:99], a[128:131], v[176:179], v[96:99]\n"
    "ds_read_b64_tr_b16 a[128:129], %[vb0] offset:16384\n"
    "v_fma_f32 v145, v145, s65, v209\n"
    "v_mfma_f32_16x16x32_bf16 v[96:99], a[132:135], v[184:187], v[96:99]\n"
    "ds_read_b64_tr_b16 a[130:131], %[vb0] offset:20480\n"
    "v_fma_f32 v146, v146, s65, v209\n"
    "v_mfma_f32_16x16x32_bf16 v[100:103], a[136:139], v[176:179], v[100:103]\n"
    "ds_read_b64_tr_b16 a[132:133], %[vb0] offset:24576\n"
    "v_fma_f32 v147, v147, s65, v209\n"
    "v_mfma_f32_16x16x32_bf16 v[100:103], a[140:143], v[184:187], v[100:103]\n"
    "ds_read_b64_tr_b16 a[134:135], %[vb0] offset:28672\n"
    "v_fma_f32 v148, v148, s65, v209\n"
    "v_mfma_f32_16x16x32_bf16 v[104:107], a[144:147], v[176:179], v[104:107]\n"
    "ds_read_b64_tr_b16 a[136:137], %[vb1] offset:16384\n"
    "v_fma_f32 v149, v149, s65, v209\n"
    "v_mfma_f32_16x16x32_bf16 v[104:107], a[148:151], v[184:187], v[104:107]\n"
    "ds_read_b64_tr_b16 a[138:139], %[vb1] offset:20480\n"
    "v_fma_f32 v150, v150, s65, v209\n"
    "v_mfma_f32_16x16x32_bf16 v[108:111], a[152:155], v[176:179], v[108:111]\n"
    "ds_read_b64_tr_b16 a[140:141], %[vb1] offset:24576\n"
    "v_fma_f32 v151, v151, s65, v209\n"
    "v_mfma_f32_16x16x32_bf16 v[108:111], a[156:159], v[184:187], v[108:111]\n"
    "ds_read_b64_tr_b16 a[142:143], %[vb1] offset:28672\n"
    "v_fma_f32 v152, v152, s65, v209\n"
    "v_mfma_f32_16x16x32_bf16 v[112:115], a[160:163], v[176:179], v[112:115]\n"
    "s_add_u32 m0, s61, 0x0\n"
    "ds_read_b64_tr_b16 a[144:145], %[vb0] offset:16640\n"
    "v_fma_f32 v153, v153, s65, v209\n"
    "v_mfma_f32_16x16x32_bf16 v[112:115], a[164:167], v[184:187], v[112:115]\n"
    "buffer_load_dwordx4 %[vo1], s[40:43], s72 offen offset:3072 nt lds\n"
    "v_exp_f32_e32 v129, v129\n"
    "v_mfma_f32_16x16x32_bf16 v[116:119], a[168:171], v[176:179], v[116:119]\n"
    "s_add_u32 s72, s72, 0x1000\n"
    "ds_read_b64_tr_b16 a[146:147], %[vb0] offset:20736\n"
    "v_fma_f32 v154, v154, s65, v209\n"
    "v_mfma_f32_16x16x32_bf16 v[116:119], a[172:175], v[184:187], v[116:119]\n"
    "ds_read_b64_tr_b16 a[148:149], %[vb0] offset:24832\n"
    "v_exp_f32_e32 v130, v130\n"
    "v_mfma_f32_16x16x32_bf16 v[120:123], a[176:179], v[176:179], v[120:123]\n"
    "s_add_u32 m0, s60, 0x1c000\n"
    "v_exp_f32_e32 v131, v131\n"
    "v_mfma_f32_16x16x32_bf16 v[120:123], a[180:183], v[184:187], v[120:123]\n"
    "buffer_load_dwordx4 %[vo3], s[48:51], s57 offen offset:2048 nt lds\n"
    "v_cvt_pk_bf16_f32 v128, v128, v129\n"
    "v_mfma_f32_16x16x32_bf16 v[124:127], a[184:187], v[176:179], v[124:127]\n"
    "v_exp_f32_e32 v132, v132\n"
    "v_mfma_f32_16x16x32_bf16 v[124:127], a[188:191], v[184:187], v[124:127]\n"
    "v_exp_f32_e32 v133, v133\n"
    "v_mfma_f32_16x16x32_bf16 v[160:163], a[64:67], a[32:35], 0\n"
    "v_exp_f32_e32 v134, v134\n"
    "v_mfma_f32_16x16x32_bf16 v[160:163], a[68:71], a[36:39], v[160:163]\n"
    "v_exp_f32_e32 v135, v135\n"
    "v_mfma_f32_16x16x32_bf16 v[160:163], a[72:75], a[40:43], v[160:163]\n"
    "v_exp_f32_e32 v136, v136\n"
    "v_mfma_f32_16x16x32_bf16 v[160:163], a[76:79], a[44:47], v[160:163]\n"
    "v_exp_f32_e32 v137, v137\n"
    "v_mfma_f32_16x16x32_bf16 v[164:167], a[80:83], a[32:35], 0\n"
    "v_exp_f32_e32 v138, v138\n"
    "v_mfma_f32_16x16x32_bf16 v[164:167], a[84:87], a[36:39], v[164:167]\n"
    "v_exp_f32_e32 v139, v139\n"
    "v_mfma_f32_16x16x32_bf16 v[164:167], a[88:91], a[40:43], v[164:167]\n"
    "v_exp_f32_e32 v140, v140\n"
    "v_mfma_f32_16x16x32_bf16 v[164:167], a[92:95], a[44:47], v[164:167]\n"
    "v_exp_f32_e32 v141, v141\n"
    "v_mfma_f32_16x16x32_bf16 v[168:171], a[96:99], a[32:35], 0\n"
    "v_exp_f32_e32 v142, v142\n"
    "v_mfma_f32_16x16x32_bf16 v[168:171], a[100:103], a[36:39], v[168:171]\n"
    "v_exp_f32_e32 v143, v143\n"
    "v_mfma_f32_16x16x32_bf16 v[168:171], a[104:107], a[40:43], v[168:171]\n"
    "ds_read_b64_tr_b16 a[150:151], %[vb0] offset:28928\n"
    "v_cvt_pk_bf16_f32 v129, v130, v131\n"
    "v_mfma_f32_16x16x32_bf16 v[168:171], a[108:111], a[44:47], v[168:171]\n"
    "ds_read_b64_tr_b16 a[152:153], %[vb1] offset:16640\n"
    "v_cvt_pk_bf16_f32 v130, v132, v133\n"
    "v_mfma_f32_16x16x32_bf16 v[172:175], a[112:115], a[32:35], 0\n"
    "ds_read_b64_tr_b16 a[154:155], %[vb1] offset:20736\n"
    "v_cvt_pk_bf16_f32 v131, v134, v135\n"
    "v_mfma_f32_16x16x32_bf16 v[172:175], a[116:119], a[36:39], v[172:175]\n"
    "v_cvt_pk_bf16_f32 v136, v136, v137\n"
    "v_cvt_pk_bf16_f32 v137, v138, v139\n"
    "v_mfma_f32_16x16x32_bf16 v[172:175], a[120:123], a[40:43], v[172:175]\n"
    "v_cvt_pk_bf16_f32 v138, v140, v141\n"
    "v_cvt_pk_bf16_f32 v139, v142, v143\n"
    "v_mfma_f32_16x16x32_bf16 v[172:175], a[124:127], a[44:47], v[172:175]\n"
    "ds_read_b64_tr_b16 a[156:157], %[vb1] offset:24832\n"
    "ds_read_b64_tr_b16 a[158:159], %[vb1] offset:28928\n"
    "v_mfma_f32_16x16x32_bf16 a[224:227], a[240:243], v[128:131], a[224:227]\n"
    "ds_read_b64_tr_b16 a[160:161], %[vb0] offset:16896\n"
    "ds_read_b64_tr_b16 a[162:163], %[vb0] offset:20992\n"
    "v_mfma_f32_16x16x32_bf16 a[224:227], a[240:243], v[136:139], a[224:227]\n"
    "s_waitcnt lgkmcnt(7)\n"
    "ds_read_b64_tr_b16 a[164:165], %[vb0] offset:25088\n"
    "ds_read_b64_tr_b16 a[166:167], %[vb0] offset:29184\n"
    "v_mfma_f32_16x16x32_bf16 v[0:3], a[128:131], v[128:131], v[0:3]\n"
    "ds_read_b64_tr_b16 a[168:169], %[vb1] offset:16896\n"
    "ds_read_b64_tr_b16 a[170:171], %[vb1] offset:20992\n"
    "v_mfma_f32_16x16x32_bf16 v[0:3], a[132:135], v[136:139], v[0:3]\n"
    "ds_read_b64_tr_b16 a[172:173], %[vb1] offset:25088\n"
    "ds_read_b64_tr_b16 a[174:175], %[vb1] offset:29184\n"
    "v_mfma_f32_16x16x32_bf16 v[4:7], a[136:139], v[128:131], v[4:7]\n"
    "ds_read_b64_tr_b16 a[176:177], %[vb0] offset:17152\n"
    "ds_read_b64_tr_b16 a[178:179], %[vb0] offset:21248\n"
    "v_mfma_f32_16x16x32_bf16 v[4:7], a[140:143], v[136:139], v[4:7]\n"
    "ds_read_b64_tr_b16 a[180:181], %[vb0] offset:25344\n"
    "ds_read_b64_tr_b16 a[182:183], %[vb0] offset:29440\n"
    "v_mfma_f32_16x16x32_bf16 v[8:11], a[144:147], v[128:131], v[8:11]\n"
    "s_waitcnt lgkmcnt(15)\n"
    "ds_read_b64_tr_b16 a[184:185], %[vb1] offset:17152\n"
    "ds_read_b64_tr_b16 a[186:187], %[vb1] offset:21248\n"
    "v_mfma_f32_16x16x32_bf16 v[8:11], a[148:151], v[136:139], v[8:11]\n"
    "s_waitcnt lgkmcnt(15)\n"
    "ds_read_b64_tr_b16 a[188:189], %[vb1] offset:25344\n"
    "ds_read_b64_tr_b16 a[190:191], %[vb1] offset:29440\n"
    "v_mfma_f32_16x16x32_bf16 v[12:15], a[152:155], v[128:131], v[12:15]\n"
    "s_waitcnt lgkmcnt(15)\n"
    "buffer_load_dwordx4 %[vo1], s[48:51], s57 offen offset:3072 nt lds\n"
    "v_fma_f32 v155, v155, s65, v209\n"
    "v_mfma_f32_16x16x32_bf16 v[12:15], a[156:159], v[136:139], v[12:15]\n"
    "s_waitcnt lgkmcnt(14)\n"
    "s_add_u32 s57, s57, 0x4000\n"
    "v_fma_f32 v156, v156, s65, v209\n"
    "v_fma_f32 v157, v157, s65, v209\n"
    "v_mfma_f32_16x16x32_bf16 v[16:19], a[160:163], v[128:131], v[16:19]\n"
    "s_waitcnt lgkmcnt(12)\n"
    "s_add_u32 m0, s61, 0x1000\n"
    "v_fma_f32 v158, v158, s65, v209\n"
    "v_fma_f32 v159, v159, s65, v209\n"
    "v_mfma_f32_16x16x32_bf16 v[16:19], a[164:167], v[136:139], v[16:19]\n"
    "s_waitcnt lgkmcnt(10)\n"
    "buffer_load_dwordx4 %[vo0], s[40:43], s72 offen nt lds\n"
    "v_fma_f32 v160, v160, s65, v210\n"
    "v_mfma_f32_16x16x32_bf16 v[20:23], a[168:171], v[128:131], v[20:23]\n"
    "s_waitcnt lgkmcnt(8)\n"
    "v_exp_f32_e32 v144, v144\n"
    "v_mfma_f32_16x16x32_bf16 v[20:23], a[172:175], v[136:139], v[20:23]\n"
    "s_waitcnt lgkmcnt(6)\n"
    "v_exp_f32_e32 v145, v145\n"
    "v_mfma_f32_16x16x32_bf16 v[24:27], a[176:179], v[128:131], v[24:27]\n"
    "s_waitcnt lgkmcnt(4)\n"
    "v_exp_f32_e32 v146, v146\n"
    "v_mfma_f32_16x16x32_bf16 v[24:27], a[180:183], v[136:139], v[24:27]\n"
    "s_waitcnt lgkmcnt(2)\n"
    "v_exp_f32_e32 v147, v147\n"
    "v_mfma_f32_16x16x32_bf16 v[28:31], a[184:187], v[128:131], v[28:31]\n"
    "s_waitcnt lgkmcnt(0)\n"
    "v_exp_f32_e32 v148, v148\n"
    "v_mfma_f32_16x16x32_bf16 v[28:31], a[188:191], v[136:139], v[28:31]\n"
    "v_exp_f32_e32 v149, v149\n"
    "s_waitcnt vmcnt(13)\n"
    ".Lpace6_%=:\n"
    "s_memrealtime s[74:75]\n"
    "s_waitcnt lgkmcnt(0)\n"
    "s_sub_u32 s74, s74, s36\n"
    "s_cmp_lt_i32 s74, s76\n"
    "s_cbranch_scc1 .Lpace6_%=\n"
    "s_barrier\n"
    "v_mfma_f32_16x16x32_bf16 v[176:179], a[64:67], a[48:51], 0\n"
    "s_sub_i32 s74, s74, 4\n"
    "v_exp_f32_e32 v150, v150\n"
    "v_mfma_f32_16x16x32_bf16 v[176:179], a[68:71], a[52:55], v[176:179]\n"
    "s_max_i32 s76, s74, s38\n"
    "v_exp_f32_e32 v151, v151\n"
    "v_mfma_f32_16x16x32_bf16 v[176:179], a[72:75], a[56:59], v[176:179]\n"
    "s_add_i32 s75, s77, -14\n"
    "v_exp_f32_e32 v152, v152\n"
    "v_mfma_f32_16x16x32_bf16 v[176:179], a[76:79], a[60:63], v[176:179]\n"
    "s_add_u32 s76, s76, s75\n"
    "v_exp_f32_e32 v153, v153\n"
    "v_mfma_f32_16x16x32_bf16 v[180:183], a[80:83], a[48:51], 0\n"
    "s_add_u32 s38, s38, s75\n"
    "v_exp_f32_e32 v154, v154\n"
    "v_mfma_f32_16x16x32_bf16 v[180:183], a[84:87], a[52:55], v[180:183]\n"
    "s_add_u32 m0, s60, 0x0\n"
    "s_sub_i32 s77, s77, 16\n"
    "v_exp_f32_e32 v155, v155\n"
    "v_mfma_f32_16x16x32_bf16 v[180:183], a[88:91], a[56:59], v[180:183]\n"
    "s_max_i32 s77, s77, s71\n"
    "buffer_load_dwordx4 %[vo0], s[44:47], s56 offen nt lds\n"
    "v_cvt_pk_bf16_f32 v144, v144, v145\n"
    "v_mfma_f32_16x16x32_bf16 v[180:183], a[92:95], a[60:63], v[180:183]\n"
    "v_exp_f32_e32 v156, v156\n"
    "v_mfma_f32_16x16x32_bf16 v[184:187], a[96:99], a[48:51], 0\n"
    "v_exp_f32_e32 v157, v157\n"
    "v_mfma_f32_16x16x32_bf16 v[184:187], a[100:103], a[52:55], v[184:187]\n"
    "v_exp_f32_e32 v158, v158\n"
    "v_mfma_f32_16x16x32_bf16 v[184:187], a[104:107], a[56:59], v[184:187]\n"
    "v_exp_f32_e32 v159, v159\n"
    "v_cvt_pk_bf16_f32 v145, v146, v147\n"
    "v_mfma_f32_16x16x32_bf16 v[184:187], a[108:111], a[60:63], v[184:187]\n"
    "v_cvt_pk_bf16_f32 v146, v148, v149\n"
    "v_cvt_pk_bf16_f32 v147, v150, v151\n"
    "v_mfma_f32_16x16x32_bf16 v[188:191], a[112:115], a[48:51], 0\n"
    "v_cvt_pk_bf16_f32 v152, v152, v153\n"
    "v_cvt_pk_bf16_f32 v153, v154, v155\n"
    "v_cvt_pk_bf16_f32 v154, v156, v157\n"
    "v_mfma_f32_16x16x32_bf16 v[188:191], a[116:119], a[52:55], v[188:191]\n"
    "ds_read_b128 a[64:67], %[kb] offset:32768\n"
    "v_cvt_pk_bf16_f32 v155, v158, v159\n"
    "v_fma_f32 v161, v161, s65, v210\n"
    "v_mfma_f32_16x16x32_bf16 v[188:191], a[120:123], a[56:59], v[188:191]\n"
    "ds_read_b128 a[68:71], %[kb] offset:33024\n"
    "v_fma_f32 v162, v162, s65, v210\n"
    "v_fma_f32 v163, v163, s65, v210\n"
    "v_mfma_f32_16x16x32_bf16 v[188:191], a[124:127], a[60:63], v[188:191]\n"
    "ds_read_b128 a[72:75], %[kb] offset:33280\n"
    "v_fma_f32 v164, v164, s65, v210\n"
    "v_fma_f32 v165, v165, s65, v210\n"
    "v_mfma_f32_16x16x32_bf16 a[228:231], a[240:243], v[144:147], a[228:231]\n"
    "ds_read_b128 a[76:79], %[kb] offset:33536\n"
    "v_fma_f32 v166, v166, s65, v210\n"
    "v_mfma_f32_16x16x32_bf16 a[228:231], a[240:243], v[152:155], a[228:231]\n"
    "ds_read_b128 a[80:83], %[kb] offset:36864\n"
    "v_fma_f32 v167, v167, s65, v210\n"
    "v_mfma_f32_16x16x32_bf16 v[32:35], a[128:131], v[144:147], v[32:35]\n"
    "ds_read_b128 a[84:87], %[kb] offset:37120\n"
    "v_fma_f32 v168, v168, s65, v210\n"
    "v_mfma_f32_16x16x32_bf16 v[32:35], a[132:135], v[152:155], v[32:35]\n"
    "ds_read_b128 a[88:91], %[kb] offset:37376\n"
    "v_fma_f32 v169, v169, s65, v210\n"
    "v_mfma_f32_16x16x32_bf16 v[36:39], a[136:139], v[144:147], v[36:39]\n"
    "ds_read_b128 a[92:95], %[kb] offset:37632\n"
    "v_fma_f32 v170, v170, s65, v210\n"
    "v_mfma_f32_16x16x32_bf16 v[36:39], a[140:143], v[152:155], v[36:39]\n"
    "ds_read_b128 a[96:99], %[kb] offset:40960\n"
    "v_fma_f32 v171, v171, s65, v210\n"
    "v_mfma_f32_16x16x32_bf16 v[40:43], a[144:147], v[144:147], v[40:43]\n"
    "ds_read_b128 a[100:103], %[kb] offset:41216\n"
    "v_fma_f32 v172, v172, s65, v210\n"
    "v_mfma_f32_16x16x32_bf16 v[40:43], a[148:151], v[152:155], v[40:43]\n"
    "ds_read_b128 a[104:107], %[kb] offset:41472\n"
    "v_fma_f32 v173, v173, s65, v210\n"
    "v_mfma_f32_16x16x32_bf16 v[44:47], a[152:155], v[144:147], v[44:47]\n"
    "ds_read_b128 a[108:111], %[kb] offset:41728\n"
    "v_fma_f32 v174, v174, s65, v210\n"
    "v_mfma_f32_16x16x32_bf16 v[44:47], a[156:159], v[152:155], v[44:47]\n"
    "ds_read_b128 a[112:115], %[kb] offset:45056\n"
    "v_fma_f32 v175, v175, s65, v210\n"
    "v_mfma_f32_16x16x32_bf16 v[48:51], a[160:163], v[144:147], v[48:51]\n"
    "ds_read_b128 a[116:119], %[kb] offset:45312\n"
    "v_fma_f32 v176, v176, s65, v211\n"
    "v_mfma_f32_16x16x32_bf16 v[48:51], a[164:167], v[152:155], v[48:51]\n"
    "buffer_load_dwordx4 %[vo2], s[44:47], s56 offen offset:1024 nt lds\n"
    "v_exp_f32_e32 v160, v160\n"
    "v_mfma_f32_16x16x32_bf16 v[52:55], a[168:171], v[144:147], v[52:55]\n"
    "s_add_u32 m0, s61, 0x1000\n"
    "ds_read_b128 a[120:123], %[kb] offset:45568\n"
    "v_fma_f32 v177, v177, s65, v211\n"
    "v_mfma_f32_16x16x32_bf16 v[52:55], a[172:175], v[152:155], v[52:55]\n"
    "buffer_load_dwordx4 %[vo2], s[40:43], s72 offen offset:1024 nt lds\n"
    "v_exp_f32_e32 v161, v161\n"
    "v_mfma_f32_16x16x32_bf16 v[56:59], a[176:179], v[144:147], v[56:59]\n"
    "ds_read_b128 a[124:127], %[kb] offset:45824\n"
    "v_fma_f32 v178, v178, s65, v211\n"
    "v_mfma_f32_16x16x32_bf16 v[56:59], a[180:183], v[152:155], v[56:59]\n"
    "v_exp_f32_e32 v162, v162\n"
    "v_cvt_pk_bf16_f32 v160, v160, v161\n"
    "v_mfma_f32_16x16x32_bf16 v[60:63], a[184:187], v[144:147], v[60:63]\n"
    "v_exp_f32_e32 v163, v163\n"
    "v_mfma_f32_16x16x32_bf16 v[60:63], a[188:191], v[152:155], v[60:63]\n"
    "s_waitcnt lgkmcnt(7)\n"
    "v_exp_f32_e32 v164, v164\n"
    "v_mfma_f32_16x16x32_bf16 v[128:131], a[64:67], a[0:3], 0\n"
    "global_load_dword v223, v222, s[78:79]\n"
    "v_cvt_pk_bf16_f32 v161, v162, v163\n"
    "v_mfma_f32_16x16x32_bf16 v[128:131], a[68:71], a[4:7], v[128:131]\n"
    "v_exp_f32_e32 v165, v165\n"
    "v_mfma_f32_16x16x32_bf16 v[128:131], a[72:75], a[8:11], v[128:131]\n"
    "v_exp_f32_e32 v166, v166\n"
    "v_mfma_f32_16x16x32_bf16 v[128:131], a[76:79], a[12:15], v[128:131]\n"
    "v_exp_f32_e32 v167, v167\n"
    "v_mfma_f32_16x16x32_bf16 v[132:135], a[80:83], a[0:3], 0\n"
    "v_exp_f32_e32 v168, v168\n"
    "v_mfma_f32_16x16x32_bf16 v[132:135], a[84:87], a[4:7], v[132:135]\n"
    "v_exp_f32_e32 v169, v169\n"
    "v_mfma_f32_16x16x32_bf16 v[132:135], a[88:91], a[8:11], v[132:135]\n"
    "v_exp_f32_e32 v170, v170\n"
    "v_mfma_f32_16x16x32_bf16 v[132:135], a[92:95], a[12:15], v[132:135]\n"
    "v_exp_f32_e32 v171, v171\n"
    "v_mfma_f32_16x16x32_bf16 v[136:139], a[96:99], a[0:3], 0\n"
    "s_waitcnt lgkmcnt(0)\n"
    "v_exp_f32_e32 v172, v172\n"
    "v_mfma_f32_16x16x32_bf16 v[136:139], a[100:103], a[4:7], v[136:139]\n"
    "v_exp_f32_e32 v173, v173\n"
    "v_mfma_f32_16x16x32_bf16 v[136:139], a[104:107], a[8:11], v[136:139]\n"
    "v_exp_f32_e32 v174, v174\n"
    "v_mfma_f32_16x16x32_bf16 v[136:139], a[108:111], a[12:15], v[136:139]\n"
    "v_exp_f32_e32 v175, v175\n"
    "v_mfma_f32_16x16x32_bf16 v[140:143], a[112:115], a[0:3], 0\n"
    "v_cvt_pk_bf16_f32 v162, v164, v165\n"
    "v_cvt_pk_bf16_f32 v163, v166, v167\n"
    "v_mfma_f32_16x16x32_bf16 v[140:143], a[116:119], a[4:7], v[140:143]\n"
    "v_cvt_pk_bf16_f32 v168, v168, v169\n"
    "v_cvt_pk_bf16_f32 v169, v170, v171\n"
    "v_mfma_f32_16x16x32_bf16 v[140:143], a[120:123], a[8:11], v[140:143]\n"
    "s_add_u32 m0, s60, 0x0\n"
    "v_cvt_pk_bf16_f32 v170, v172, v173\n"
    "v_cvt_pk_bf16_f32 v171, v174, v175\n"
    "v_mfma_f32_16x16x32_bf16 v[140:143], a[124:127], a[12:15], v[140:143]\n"
    "buffer_load_dwordx4 %[vo3], s[44:47], s56 offen offset:2048 nt lds\n"
    "v_fma_f32 v179, v179, s65, v211\n"
    "v_mfma_f32_16x16x32_bf16 a[232:235], a[240:243], v[160:163], a[232:235]\n"
    "v_fma_f32 v180, v180, s65, v211\n"
    "v_fma_f32 v181, v181, s65, v211\n"
    "v_mfma_f32_16x16x32_bf16 a[232:235], a[240:243], v[168:171], a[232:235]\n"
    "v_fma_f32 v182, v182, s65, v211\n"
    "v_fma_f32 v183, v183, s65, v211\n"
    "v_mfma_f32_16x16x32_bf16 v[64:67], a[128:131], v[160:163], v[64:67]\n"
    "v_fma_f32 v184, v184, s65, v211\n"
    "v_fma_f32 v185, v185, s65, v211\n"
    "v_mfma_f32_16x16x32_bf16 v[64:67], a[132:135], v[168:171], v[64:67]\n"
    "v_fma_f32 v186, v186, s65, v211\n"
    "v_fma_f32 v187, v187, s65, v211\n"
    "v_mfma_f32_16x16x32_bf16 v[68:71], a[136:139], v[160:163], v[68:71]\n"
    "v_fma_f32 v188, v188, s65, v211\n"
    "v_fma_f32 v189, v189, s65, v211\n"
    "v_mfma_f32_16x16x32_bf16 v[68:71], a[140:143], v[168:171], v[68:71]\n"
    "v_fma_f32 v190, v190, s65, v211\n"
    "v_fma_f32 v191, v191, s65, v211\n"
    "v_mfma_f32_16x16x32_bf16 v[72:75], a[144:147], v[160:163], v[72:75]\n"
    "v_exp_f32_e32 v176, v176\n"
    "v_mfma_f32_16x16x32_bf16 v[72:75], a[148:151], v[168:171], v[72:75]\n"
    "v_exp_f32_e32 v177, v177\n"
    "v_mfma_f32_16x16x32_bf16 v[76:79], a[152:155], v[160:163], v[76:79]\n"
    "v_exp_f32_e32 v178, v178\n"
    "v_mfma_f32_16x16x32_bf16 v[76:79], a[156:159], v[168:171], v[76:79]\n"
    "v_exp_f32_e32 v179, v179\n"
    "v_mfma_f32_16x16x32_bf16 v[80:83], a[160:163], v[160:163], v[80:83]\n"
    "s_add_u32 m0, s61, 0x1000\n"
    "v_exp_f32_e32 v180, v180\n"
    "v_mfma_f32_16x16x32_bf16 v[80:83], a[164:167], v[168:171], v[80:83]\n"
    "buffer_load_dwordx4 %[vo3], s[40:43], s72 offen offset:2048 nt lds\n"
    "v_cvt_pk_bf16_f32 v176, v176, v177\n"
    "v_mfma_f32_16x16x32_bf16 v[84:87], a[168:171], v[160:163], v[84:87]\n"
    "v_exp_f32_e32 v181, v181\n"
    "v_mfma_f32_16x16x32_bf16 v[84:87], a[172:175], v[168:171], v[84:87]\n"
    "v_exp_f32_e32 v182, v182\n"
    "v_mfma_f32_16x16x32_bf16 v[88:91], a[176:179], v[160:163], v[88:91]\n"
    "v_exp_f32_e32 v183, v183\n"
    "v_mfma_f32_16x16x32_bf16 v[88:91], a[180:183], v[168:171], v[88:91]\n"
    "v_exp_f32_e32 v184, v184\n"
    "v_mfma_f32_16x16x32_bf16 v[92:95], a[184:187], v[160:163], v[92:95]\n"
    "v_exp_f32_e32 v185, v185\n"
    "v_mfma_f32_16x16x32_bf16 v[92:95], a[188:191], v[168:171], v[92:95]\n"
    "s_add_u32 m0, s60, 0x0\n"
    "v_exp_f32_e32 v186, v186\n"
    "v_mfma_f32_16x16x32_bf16 v[144:147], a[64:67], a[16:19], 0\n"
    "buffer_load_dwordx4 %[vo1], s[44:47], s56 offen offset:3072 nt lds\n"
    "v_cvt_pk_bf16_f32 v177, v178, v179\n"
    "v_mfma_f32_16x16x32_bf16 v[144:147], a[68:71], a[20:23], v[144:147]\n"
    "s_add_u32 s56, s56, 0x4000\n"
    "v_exp_f32_e32 v187, v187\n"
    "v_mfma_f32_16x16x32_bf16 v[144:147], a[72:75], a[24:27], v[144:147]\n"
    "v_exp_f32_e32 v188, v188\n"
    "v_mfma_f32_16x16x32_bf16 v[144:147], a[76:79], a[28:31], v[144:147]\n"
    "v_exp_f32_e32 v189, v189\n"
    "v_mfma_f32_16x16x32_bf16 v[148:151], a[80:83], a[16:19], 0\n"
    "v_exp_f32_e32 v190, v190\n"
    "v_mfma_f32_16x16x32_bf16 v[148:151], a[84:87], a[20:23], v[148:151]\n"
    "v_exp_f32_e32 v191, v191\n"
    "v_mfma_f32_16x16x32_bf16 v[148:151], a[88:91], a[24:27], v[148:151]\n"
    "v_cvt_pk_bf16_f32 v178, v180, v181\n"
    "v_cvt_pk_bf16_f32 v179, v182, v183\n"
    "v_mfma_f32_16x16x32_bf16 v[148:151], a[92:95], a[28:31], v[148:151]\n"
    "v_cvt_pk_bf16_f32 v184, v184, v185\n"
    "v_cvt_pk_bf16_f32 v185, v186, v187\n"
    "v_mfma_f32_16x16x32_bf16 v[152:155], a[96:99], a[16:19], 0\n"
    "v_cvt_pk_bf16_f32 v186, v188, v189\n"
    "v_cvt_pk_bf16_f32 v187, v190, v191\n"
    "v_mfma_f32_16x16x32_bf16 v[152:155], a[100:103], a[20:23], v[152:155]\n"
    "v_fma_f32 v128, v128, s65, v208\n"
    "v_fma_f32 v129, v129, s65, v208\n"
    "v_mfma_f32_16x16x32_bf16 v[152:155], a[104:107], a[24:27], v[152:155]\n"
    "v_fma_f32 v130, v130, s65, v208\n"
    "v_fma_f32 v131, v131, s65, v208\n"
    "v_mfma_f32_16x16x32_bf16 v[152:155], a[108:111], a[28:31], v[152:155]\n"
    "v_fma_f32 v132, v132, s65, v208\n"
    "v_fma_f32 v133, v133, s65, v208\n"
    "v_mfma_f32_16x16x32_bf16 v[156:159], a[112:115], a[16:19], 0\n"
    "v_fma_f32 v134, v134, s65, v208\n"
    "v_fma_f32 v135, v135, s65, v208\n"
    "v_mfma_f32_16x16x32_bf16 v[156:159], a[116:119], a[20:23], v[156:159]\n"
    "v_fma_f32 v136, v136, s65, v208\n"
    "v_fma_f32 v137, v137, s65, v208\n"
    "v_mfma_f32_16x16x32_bf16 v[156:159], a[120:123], a[24:27], v[156:159]\n"
    "v_fma_f32 v138, v138, s65, v208\n"
    "v_fma_f32 v139, v139, s65, v208\n"
    "v_mfma_f32_16x16x32_bf16 v[156:159], a[124:127], a[28:31], v[156:159]\n"
    "v_fma_f32 v140, v140, s65, v208\n"
    "v_fma_f32 v141, v141, s65, v208\n"
    "v_mfma_f32_16x16x32_bf16 a[236:239], a[240:243], v[176:179], a[236:239]\n"
    "v_fma_f32 v142, v142, s65, v208\n"
    "v_fma_f32 v143, v143, s65, v208\n"
    "v_mfma_f32_16x16x32_bf16 a[236:239], a[240:243], v[184:187], a[236:239]\n"
    "v_exp_f32_e32 v128, v128\n"
    "v_mfma_f32_16x16x32_bf16 v[96:99], a[128:131], v[176:179], v[96:99]\n"
    "ds_read_b64_tr_b16 a[128:129], %[vb0] offset:32768\n"
    "v_fma_f32 v144, v144, s65, v209\n"
    "v_mfma_f32_16x16x32_bf16 v[96:99], a[132:135], v[184:187], v[96:99]\n"
    "ds_read_b64_tr_b16 a[130:131], %[vb0] offset:36864\n"
    "v_fma_f32 v145, v145, s65, v209\n"
    "v_mfma_f32_16x16x32_bf16 v[100:103], a[136:139], v[176:179], v[100:103]\n"
    "s_add_u32 m0, s60, 0x10000\n"
    "ds_read_b64_tr_b16 a[132:133], %[vb0] offset:40960\n"
    "v_fma_f32 v146, v146, s65, v209\n"
    "v_mfma_f32_16x16x32_bf16 v[100:103], a[140:143], v[184:187], v[100:103]\n"
    "buffer_load_dwordx4 %[vo0], s[48:51], s57 offen nt lds\n"
    "v_fma_f32 v147, v147, s65, v209\n"
    "v_mfma_f32_16x16x32_bf16 v[104:107], a[144:147], v[176:179], v[104:107]\n"
    "ds_read_b64_tr_b16 a[134:135], %[vb0] offset:45056\n"
    "v_fma_f32 v148, v148, s65, v209\n"
    "v_mfma_f32_16x16x32_bf16 v[104:107], a[148:151], v[184:187], v[104:107]\n"
    "ds_read_b64_tr_b16 a[136:137], %[vb1] offset:32768\n"
    "v_fma_f32 v149, v149, s65, v209\n"
    "v_mfma_f32_16x16x32_bf16 v[108:111], a[152:155], v[176:179], v[108:111]\n"
    "ds_read_b64_tr_b16 a[138:139], %[vb1] offset:36864\n"
    "v_fma_f32 v150, v150, s65, v209\n"
    "v_mfma_f32_16x16x32_bf16 v[108:111], a[156:159], v[184:187], v[108:111]\n"
    "ds_read_b64_tr_b16 a[140:141], %[vb1] offset:40960\n"
    "v_exp_f32_e32 v129, v129\n"
    "v_mfma_f32_16x16x32_bf16 v[112:115], a[160:163], v[176:179], v[112:115]\n"
    "s_add_u32 m0, s61, 0x1000\n"
    "ds_read_b64_tr_b16 a[142:143], %[vb1] offset:45056\n"
    "v_fma_f32 v151, v151, s65, v209\n"
    "v_mfma_f32_16x16x32_bf16 v[112:115], a[164:167], v[184:187], v[112:115]\n"
    "buffer_load_dwordx4 %[vo1], s[40:43], s72 offen offset:3072 nt lds\n"
    "v_exp_f32_e32 v130, v130\n"
    "v_mfma_f32_16x16x32_bf16 v[116:119], a[168:171], v[176:179], v[116:119]\n"
    "s_add_u32 s72, s72, 0xf000\n"
    "ds_read_b64_tr_b16 a[144:145], %[vb0] offset:33024\n"
    "v_cvt_pk_bf16_f32 v128, v128, v129\n"
    "v_mfma_f32_16x16x32_bf16 v[116:119], a[172:175], v[184:187], v[116:119]\n"
    "ds_read_b64_tr_b16 a[146:147], %[vb0] offset:37120\n"
    "v_fma_f32 v152, v152, s65, v209\n"
    "v_mfma_f32_16x16x32_bf16 v[120:123], a[176:179], v[176:179], v[120:123]\n"
    "ds_read_b64_tr_b16 a[148:149], %[vb0] offset:41216\n"
    "v_exp_f32_e32 v131, v131\n"
    "v_mfma_f32_16x16x32_bf16 v[120:123], a[180:183], v[184:187], v[120:123]\n"
    "v_exp_f32_e32 v132, v132\n"
    "v_mfma_f32_16x16x32_bf16 v[124:127], a[184:187], v[176:179], v[124:127]\n"
    "v_exp_f32_e32 v133, v133\n"
    "v_mfma_f32_16x16x32_bf16 v[124:127], a[188:191], v[184:187], v[124:127]\n"
    "v_exp_f32_e32 v134, v134\n"
    "v_mfma_f32_16x16x32_bf16 v[160:163], a[64:67], a[32:35], 0\n"
    "v_exp_f32_e32 v135, v135\n"
    "v_mfma_f32_16x16x32_bf16 v[160:163], a[68:71], a[36:39], v[160:163]\n"
    "s_add_u32 m0, s60, 0x10000\n"
    "v_exp_f32_e32 v136, v136\n"
    "v_mfma_f32_16x16x32_bf16 v[160:163], a[72:75], a[40:43], v[160:163]\n"
    "buffer_load_dwordx4 %[vo2], s[48:51], s57 offen offset:1024 nt lds\n"
    "v_cvt_pk_bf16_f32 v129, v130, v131\n"
    "v_mfma_f32_16x16x32_bf16 v[160:163], a[76:79], a[44:47], v[160:163]\n"
    "v_exp_f32_e32 v137, v137\n"
    "v_mfma_f32_16x16x32_bf16 v[164:167], a[80:83], a[32:35], 0\n"
    "v_exp_f32_e32 v138, v138\n"
    "v_mfma_f32_16x16x32_bf16 v[164:167], a[84:87], a[36:39], v[164:167]\n"
    "v_exp_f32_e32 v139, v139\n"
    "v_mfma_f32_16x16x32_bf16 v[164:167], a[88:91], a[40:43], v[164:167]\n"
    "v_exp_f32_e32 v140, v140\n"
    "v_mfma_f32_16x16x32_bf16 v[164:167], a[92:95], a[44:47], v[164:167]\n"
    "v_exp_f32_e32 v141, v141\n"
    "v_mfma_f32_16x16x32_bf16 v[168:171], a[96:99], a[32:35], 0\n"
    "v_exp_f32_e32 v142, v142\n"
    "v_mfma_f32_16x16x32_bf16 v[168:171], a[100:103], a[36:39], v[168:171]\n"
    "v_exp_f32_e32 v143, v143\n"
    "v_mfma_f32_16x16x32_bf16 v[168:171], a[104:107], a[40:43], v[168:171]\n"
    "ds_read_b64_tr_b16 a[150:151], %[vb0] offset:45312\n"
    "v_cvt_pk_bf16_f32 v130, v132, v133\n"
    "v_mfma_f32_16x16x32_bf16 v[168:171], a[108:111], a[44:47], v[168:171]\n"
    "ds_read_b64_tr_b16 a[152:153], %[vb1] offset:33024\n"
    "v_cvt_pk_bf16_f32 v131, v134, v135\n"
    "v_mfma_f32_16x16x32_bf16 v[172:175], a[112:115], a[32:35], 0\n"
    "ds_read_b64_tr_b16 a[154:155], %[vb1] offset:37120\n"
    "v_cvt_pk_bf16_f32 v136, v136, v137\n"
    "v_mfma_f32_16x16x32_bf16 v[172:175], a[116:119], a[36:39], v[172:175]\n"
    "v_cvt_pk_bf16_f32 v137, v138, v139\n"
    "v_cvt_pk_bf16_f32 v138, v140, v141\n"
    "v_mfma_f32_16x16x32_bf16 v[172:175], a[120:123], a[40:43], v[172:175]\n"
    "ds_read_b64_tr_b16 a[156:157], %[vb1] offset:41216\n"
    "v_cvt_pk_bf16_f32 v139, v142, v143\n"
    "v_mfma_f32_16x16x32_bf16 v[172:175], a[124:127], a[44:47], v[172:175]\n"
    "ds_read_b64_tr_b16 a[158:159], %[vb1] offset:45312\n"
    "v_fma_f32 v153, v153, s65, v209\n"
    "v_mfma_f32_16x16x32_bf16 a[224:227], a[240:243], v[128:131], a[224:227]\n"
    "ds_read_b64_tr_b16 a[160:161], %[vb0] offset:33280\n"
    "ds_read_b64_tr_b16 a[162:163], %[vb0] offset:37376\n"
    "v_fma_f32 v154, v154, s65, v209\n"
    "v_mfma_f32_16x16x32_bf16 a[224:227], a[240:243], v[136:139], a[224:227]\n"
    "s_waitcnt lgkmcnt(7)\n"
    "ds_read_b64_tr_b16 a[164:165], %[vb0] offset:41472\n"
    "ds_read_b64_tr_b16 a[166:167], %[vb0] offset:45568\n"
    "v_fma_f32 v155, v155, s65, v209\n"
    "v_mfma_f32_16x16x32_bf16 v[0:3], a[128:131], v[128:131], v[0:3]\n"
    "ds_read_b64_tr_b16 a[168:169], %[vb1] offset:33280\n"
    "ds_read_b64_tr_b16 a[170:171], %[vb1] offset:37376\n"
    "v_fma_f32 v156, v156, s65, v209\n"
    "v_mfma_f32_16x16x32_bf16 v[0:3], a[132:135], v[136:139], v[0:3]\n"
    "ds_read_b64_tr_b16 a[172:173], %[vb1] offset:41472\n"
    "ds_read_b64_tr_b16 a[174:175], %[vb1] offset:45568\n"
    "v_fma_f32 v157, v157, s65, v209\n"
    "v_mfma_f32_16x16x32_bf16 v[4:7], a[136:139], v[128:131], v[4:7]\n"
    "buffer_load_dwordx4 %[vo3], s[48:51], s57 offen offset:2048 nt lds\n"
    "ds_read_b64_tr_b16 a[176:177], %[vb0] offset:33536\n"
    "ds_read_b64_tr_b16 a[178:179], %[vb0] offset:37632\n"
    "v_mfma_f32_16x16x32_bf16 v[4:7], a[140:143], v[136:139], v[4:7]\n"
    "ds_read_b64_tr_b16 a[180:181], %[vb0] offset:41728\n"
    "ds_read_b64_tr_b16 a[182:183], %[vb0] offset:45824\n"
    "v_mfma_f32_16x16x32_bf16 v[8:11], a[144:147], v[128:131], v[8:11]\n"
    "s_waitcnt lgkmcnt(15)\n"
    "ds_read_b64_tr_b16 a[184:185], %[vb1] offset:33536\n"
    "ds_read_b64_tr_b16 a[186:187], %[vb1] offset:37632\n"
    "v_mfma_f32_16x16x32_bf16 v[8:11], a[148:151], v[136:139], v[8:11]\n"
    "s_waitcnt lgkmcnt(15)\n"
    "ds_read_b64_tr_b16 a[188:189], %[vb1] offset:41728\n"
    "ds_read_b64_tr_b16 a[190:191], %[vb1] offset:45824\n"
    "v_mfma_f32_16x16x32_bf16 v[12:15], a[152:155], v[128:131], v[12:15]\n"
    "s_waitcnt lgkmcnt(15)\n"
    "v_fma_f32 v158, v158, s65, v209\n"
    "v_fma_f32 v159, v159, s65, v209\n"
    "v_mfma_f32_16x16x32_bf16 v[12:15], a[156:159], v[136:139], v[12:15]\n"
    "s_waitcnt lgkmcnt(14)\n"
    "v_exp_f32_e32 v144, v144\n"
    "v_mfma_f32_16x16x32_bf16 v[16:19], a[160:163], v[128:131], v[16:19]\n"
    "s_waitcnt lgkmcnt(12)\n"
    "buffer_load_dwordx4 %[vo1], s[48:51], s57 offen offset:3072 nt lds\n"
    "v_fma_f32 v160, v160, s65, v210\n"
    "v_mfma_f32_16x16x32_bf16 v[16:19], a[164:167], v[136:139], v[16:19]\n"
    "s_waitcnt lgkmcnt(10)\n"
    "s_add_u32 s57, s57, 0x4000\n"
    "v_exp_f32_e32 v145, v145\n"
    "v_mfma_f32_16x16x32_bf16 v[20:23], a[168:171], v[128:131], v[20:23]\n"
    "s_waitcnt lgkmcnt(8)\n"
    "v_exp_f32_e32 v146, v146\n"
    "v_mfma_f32_16x16x32_bf16 v[20:23], a[172:175], v[136:139], v[20:23]\n"
    "s_waitcnt lgkmcnt(6)\n"
    "v_exp_f32_e32 v147, v147\n"
    "v_mfma_f32_16x16x32_bf16 v[24:27], a[176:179], v[128:131], v[24:27]\n"
    "s_waitcnt lgkmcnt(4)\n"
    "v_exp_f32_e32 v148, v148\n"
    "v_mfma_f32_16x16x32_bf16 v[24:27], a[180:183], v[136:139], v[24:27]\n"
    "s_waitcnt lgkmcnt(2)\n"
    "v_exp_f32_e32 v149, v149\n"
    "v_mfma_f32_16x16x32_bf16 v[28:31], a[184:187], v[128:131], v[28:31]\n"
    "s_waitcnt lgkmcnt(0)\n"
    "v_exp_f32_e32 v150, v150\n"
    "v_mfma_f32_16x16x32_bf16 v[28:31], a[188:191], v[136:139], v[28:31]\n"
    "v_exp_f32_e32 v151, v151\n"
    "s_waitcnt vmcnt(13)\n"
    ".Lpace7_%=:\n"
    "s_memrealtime s[74:75]\n"
    "s_waitcnt lgkmcnt(0)\n"
    "s_sub_u32 s74, s74, s36\n"
    "s_cmp_lt_i32 s74, s76\n"
    "s_cbranch_scc1 .Lpace7_%=\n"
    "s_barrier\n"
    "v_mfma_f32_16x16x32_bf16 v[176:179], a[64:67], a[48:51], 0\n"
    "s_sub_i32 s74, s74, 4\n"
    "v_exp_f32_e32 v152, v152\n"
    "v_cvt_pk_bf16_f32 v144, v144, v145\n"
    "v_mfma_f32_16x16x32_bf16 v[176:179], a[68:71], a[52:55], v[176:179]\n"
    "s_max_i32 s76, s74, s38\n"
    "v_exp_f32_e32 v153, v153\n"
    "v_mfma_f32_16x16x32_bf16 v[176:179], a[72:75], a[56:59], v[176:179]\n"
    "s_add_i32 s75, s77, 8\n"
    "v_exp_f32_e32 v154, v154\n"
    "v_mfma_f32_16x16x32_bf16 v[176:179], a[76:79], a[60:63], v[176:179]\n"
    "s_add_u32 m0, s60, 0x4000\n"
    "s_add_u32 s76, s76, s75\n"
    "v_exp_f32_e32 v155, v155\n"
    "v_mfma_f32_16x16x32_bf16 v[180:183], a[80:83], a[48:51], 0\n"
    "s_add_u32 s38, s38, s75\n"
    "buffer_load_dwordx4 %[vo0], s[44:47], s56 offen nt lds\n"
    "v_cvt_pk_bf16_f32 v145, v146, v147\n"
    "v_mfma_f32_16x16x32_bf16 v[180:183], a[84:87], a[52:55], v[180:183]\n"
    "s_sub_i32 s77, s77, 16\n"
    "v_exp_f32_e32 v156, v156\n"
    "v_mfma_f32_16x16x32_bf16 v[180:183], a[88:91], a[56:59], v[180:183]\n"
    "s_max_i32 s77, s77, s71\n"
    "v_exp_f32_e32 v157, v157\n"
    "v_mfma_f32_16x16x32_bf16 v[180:183], a[92:95], a[60:63], v[180:183]\n"
    "v_exp_f32_e32 v158, v158\n"
    "v_mfma_f32_16x16x32_bf16 v[184:187], a[96:99], a[48:51], 0\n"
    "v_exp_f32_e32 v159, v159\n"
    "v_mfma_f32_16x16x32_bf16 v[184:187], a[100:103], a[52:55], v[184:187]\n"
    "v_cvt_pk_bf16_f32 v146, v148, v149\n"
    "v_cvt_pk_bf16_f32 v147, v150, v151\n"
    "v_mfma_f32_16x16x32_bf16 v[184:187], a[104:107], a[56:59], v[184:187]\n"
    "v_cvt_pk_bf16_f32 v152, v152, v153\n"
    "v_cvt_pk_bf16_f32 v153, v154, v155\n"
    "v_mfma_f32_16x16x32_bf16 v[184:187], a[108:111], a[60:63], v[184:187]\n"
    "v_cvt_pk_bf16_f32 v154, v156, v157\n"
    "v_cvt_pk_bf16_f32 v155, v158, v159\n"
    "v_mfma_f32_16x16x32_bf16 v[188:191], a[112:115], a[48:51], 0\n"
    "ds_read_b128 a[64:67], %[kb] offset:49152\n"
    "v_fma_f32 v161, v161, s65, v210\n"
    "v_mfma_f32_16x16x32_bf16 v[188:191], a[116:119], a[52:55], v[188:191]\n"
    "ds_read_b128 a[68:71], %[kb] offset:49408\n"
    "v_fma_f32 v162, v162, s65, v210\n"
    "v_mfma_f32_16x16x32_bf16 v[188:191], a[120:123], a[56:59], v[188:191]\n"
    "ds_read_b128 a[72:75], %[kb] offset:49664\n"
    "v_fma_f32 v163, v163, s65, v210\n"
    "v_mfma_f32_16x16x32_bf16 v[188:191], a[124:127], a[60:63], v[188:191]\n"
    "buffer_load_dwordx4 %[vo2], s[44:47], s56 offen offset:1024 nt lds\n"
    "v_fma_f32 v164, v164, s65, v210\n"
    "v_mfma_f32_16x16x32_bf16 a[228:231], a[240:243], v[144:147], a[228:231]\n"
    "ds_read_b128 a[76:79], %[kb] offset:49920\n"
    "v_fma_f32 v165, v165, s65, v210\n"
    "v_mfma_f32_16x16x32_bf16 a[228:231], a[240:243], v[152:155], a[228:231]\n"
    "ds_read_b128 a[80:83], %[kb] offset:53248\n"
    "v_fma_f32 v166, v166, s65, v210\n"
    "v_mfma_f32_16x16x32_bf16 v[32:35], a[128:131], v[144:147], v[32:35]\n"
    "ds_read_b128 a[84:87], %[kb] offset:53504\n"
    "v_fma_f32 v167, v167, s65, v210\n"
    "v_mfma_f32_16x16x32_bf16 v[32:35], a[132:135], v[152:155], v[32:35]\n"
    "ds_read_b128 a[88:91], %[kb] offset:53760\n"
    "v_fma_f32 v168, v168, s65, v210\n"
    "v_mfma_f32_16x16x32_bf16 v[36:39], a[136:139], v[144:147], v[36:39]\n"
    "ds_read_b128 a[92:95], %[kb] offset:54016\n"
    "v_fma_f32 v169, v169, s65, v210\n"
    "v_mfma_f32_16x16x32_bf16 v[36:39], a[140:143], v[152:155], v[36:39]\n"
    "ds_read_b128 a[96:99], %[kb] offset:57344\n"
    "v_fma_f32 v170, v170, s65, v210\n"
    "v_mfma_f32_16x16x32_bf16 v[40:43], a[144:147], v[144:147], v[40:43]\n"
    "ds_read_b128 a[100:103], %[kb] offset:57600\n"
    "v_fma_f32 v171, v171, s65, v210\n"
    "v_mfma_f32_16x16x32_bf16 v[40:43], a[148:151], v[152:155], v[40:43]\n"
    "ds_read_b128 a[104:107], %[kb] offset:57856\n"
    "v_fma_f32 v172, v172, s65, v210\n"
    "v_mfma_f32_16x16x32_bf16 v[44:47], a[152:155], v[144:147], v[44:47]\n"
    "ds_read_b128 a[108:111], %[kb] offset:58112\n"
    "v_fma_f32 v173, v173, s65, v210\n"
    "v_mfma_f32_16x16x32_bf16 v[44:47], a[156:159], v[152:155], v[44:47]\n"
    "ds_read_b128 a[112:115], %[kb] offset:61440\n"
    "v_fma_f32 v174, v174, s65, v210\n"
    "v_mfma_f32_16x16x32_bf16 v[48:51], a[160:163], v[144:147], v[48:51]\n"
    "buffer_load_dwordx4 %[vo3], s[44:47], s56 offen offset:2048 nt lds\n"
    "v_fma_f32 v175, v175, s65, v210\n"
    "v_mfma_f32_16x16x32_bf16 v[48:51], a[164:167], v[152:155], v[48:51]\n"
    "ds_read_b128 a[116:119], %[kb] offset:61696\n"
    "v_fma_f32 v176, v176, s65, v211\n"
    "v_mfma_f32_16x16x32_bf16 v[52:55], a[168:171], v[144:147], v[52:55]\n"
    "ds_read_b128 a[120:123], %[kb] offset:61952\n"
    "v_exp_f32_e32 v160, v160\n"
    "v_mfma_f32_16x16x32_bf16 v[52:55], a[172:175], v[152:155], v[52:55]\n"
    "ds_read_b128 a[124:127], %[kb] offset:62208\n"
    "v_fma_f32 v177, v177, s65, v211\n"
    "v_mfma_f32_16x16x32_bf16 v[56:59], a[176:179], v[144:147], v[56:59]\n"
    "v_exp_f32_e32 v161, v161\n"
    "v_fma_f32 v178, v178, s65, v211\n"
    "v_mfma_f32_16x16x32_bf16 v[56:59], a[180:183], v[152:155], v[56:59]\n"
    "v_exp_f32_e32 v162, v162\n"
    "v_fma_f32 v179, v179, s65, v211\n"
    "v_mfma_f32_16x16x32_bf16 v[60:63], a[184:187], v[144:147], v[60:63]\n"
    "v_exp_f32_e32 v163, v163\n"
    "v_cvt_pk_bf16_f32 v160, v160, v161\n"
    "v_mfma_f32_16x16x32_bf16 v[60:63], a[188:191], v[152:155], v[60:63]\n"
    "s_waitcnt lgkmcnt(7)\n"
    "v_exp_f32_e32 v164, v164\n"
    "v_mfma_f32_16x16x32_bf16 v[128:131], a[64:67], a[0:3], 0\n"
    "v_exp_f32_e32 v165, v165\n"
    "v_mfma_f32_16x16x32_bf16 v[128:131], a[68:71], a[4:7], v[128:131]\n"
    "v_exp_f32_e32 v166, v166\n"
    "v_mfma_f32_16x16x32_bf16 v[128:131], a[72:75], a[8:11], v[128:131]\n"
    "buffer_load_dwordx4 %[vo1], s[44:47], s56 offen offset:3072 nt lds\n"
    "v_cvt_pk_bf16_f32 v161, v162, v163\n"
    "v_mfma_f32_16x16x32_bf16 v[128:131], a[76:79], a[12:15], v[128:131]\n"
    "s_add_u32 s56, s56, 0x4000\n"
    "v_exp_f32_e32 v167, v167\n"
    "v_mfma_f32_16x16x32_bf16 v[132:135], a[80:83], a[0:3], 0\n"
    "v_exp_f32_e32 v168, v168\n"
    "v_mfma_f32_16x16x32_bf16 v[132:135], a[84:87], a[4:7], v[132:135]\n"
    "v_exp_f32_e32 v169, v169\n"
    "v_mfma_f32_16x16x32_bf16 v[132:135], a[88:91], a[8:11], v[132:135]\n"
    "v_exp_f32_e32 v170, v170\n"
    "v_mfma_f32_16x16x32_bf16 v[132:135], a[92:95], a[12:15], v[132:135]\n"
    "v_exp_f32_e32 v171, v171\n"
    "v_mfma_f32_16x16x32_bf16 v[136:139], a[96:99], a[0:3], 0\n"
    "s_waitcnt lgkmcnt(0)\n"
    "v_exp_f32_e32 v172, v172\n"
    "v_mfma_f32_16x16x32_bf16 v[136:139], a[100:103], a[4:7], v[136:139]\n"
    "v_exp_f32_e32 v173, v173\n"
    "v_mfma_f32_16x16x32_bf16 v[136:139], a[104:107], a[8:11], v[136:139]\n"
    "v_exp_f32_e32 v174, v174\n"
    "v_mfma_f32_16x16x32_bf16 v[136:139], a[108:111], a[12:15], v[136:139]\n"
    "v_exp_f32_e32 v175, v175\n"
    "v_mfma_f32_16x16x32_bf16 v[140:143], a[112:115], a[0:3], 0\n"
    "v_cvt_pk_bf16_f32 v162, v164, v165\n"
    "v_cvt_pk_bf16_f32 v163, v166, v167\n"
    "v_mfma_f32_16x16x32_bf16 v[140:143], a[116:119], a[4:7], v[140:143]\n"
    "v_cvt_pk_bf16_f32 v168, v168, v169\n"
    "v_cvt_pk_bf16_f32 v169, v170, v171\n"
    "v_mfma_f32_16x16x32_bf16 v[140:143], a[120:123], a[8:11], v[140:143]\n"
    "s_add_u32 m0, s60, 0x14000\n"
    "v_cvt_pk_bf16_f32 v170, v172, v173\n"
    "v_cvt_pk_bf16_f32 v171, v174, v175\n"
    "v_mfma_f32_16x16x32_bf16 v[140:143], a[124:127], a[12:15], v[140:143]\n"
    "buffer_load_dwordx4 %[vo0], s[48:51], s57 offen nt lds\n"
    "v_fma_f32 v180, v180, s65, v211\n"
    "v_mfma_f32_16x16x32_bf16 a[232:235], a[240:243], v[160:163], a[232:235]\n"
    "v_fma_f32 v181, v181, s65, v211\n"
    "v_fma_f32 v182, v182, s65, v211\n"
    "v_mfma_f32_16x16x32_bf16 a[232:235], a[240:243], v[168:171], a[232:235]\n"
    "v_fma_f32 v183, v183, s65, v211\n"
    "v_fma_f32 v184, v184, s65, v211\n"
    "v_mfma_f32_16x16x32_bf16 v[64:67], a[128:131], v[160:163], v[64:67]\n"
    "v_fma_f32 v185, v185, s65, v211\n"
    "v_fma_f32 v186, v186, s65, v211\n"
    "v_fma_f32 v187, v187, s65, v211\n"
    "v_fma_f32 v188, v188, s65, v211\n"
    "v_fma_f32 v189, v189, s65, v211\n"
    "v_fma_f32 v190, v190, s65, v211\n"
    "v_mfma_f32_16x16x32_bf16 v[64:67], a[132:135], v[168:171], v[64:67]\n"
    "v_fma_f32 v191, v191, s65, v211\n"
    "v_exp_f32_e32 v176, v176\n"
    "v_exp_f32_e32 v177, v177\n"
    "v_fma_f32 v128, v128, s65, v208\n"
    "v_mfma_f32_16x16x32_bf16 v[68:71], a[136:139], v[160:163], v[68:71]\n"
    "v_exp_f32_e32 v178, v178\n"
    "v_exp_f32_e32 v179, v179\n"
    "v_mfma_f32_16x16x32_bf16 v[68:71], a[140:143], v[168:171], v[68:71]\n"
    "v_exp_f32_e32 v180, v180\n"
    "v_exp_f32_e32 v181, v181\n"
    "v_mfma_f32_16x16x32_bf16 v[72:75], a[144:147], v[160:163], v[72:75]\n"
    "v_exp_f32_e32 v182, v182\n"
    "v_exp_f32_e32 v183, v183\n"
    "v_mfma_f32_16x16x32_bf16 v[72:75], a[148:151], v[168:171], v[72:75]\n"
    "v_exp_f32_e32 v184, v184\n"
    "v_exp_f32_e32 v185, v185\n"
    "v_mfma_f32_16x16x32_bf16 v[76:79], a[152:155], v[160:163], v[76:79]\n"
    "buffer_load_dwordx4 %[vo2], s[48:51], s57 offen offset:1024 nt lds\n"
    "v_exp_f32_e32 v186, v186\n"
    "v_mfma_f32_16x16x32_bf16 v[76:79], a[156:159], v[168:171], v[76:79]\n"
    "v_exp_f32_e32 v187, v187\n"
    "v_cvt_pk_bf16_f32 v176, v176, v177\n"
    "v_mfma_f32_16x16x32_bf16 v[80:83], a[160:163], v[160:163], v[80:83]\n"
    "v_exp_f32_e32 v188, v188\n"
    "v_cvt_pk_bf16_f32 v177, v178, v179\n"
    "v_mfma_f32_16x16x32_bf16 v[80:83], a[164:167], v[168:171], v[80:83]\n"
    "v_exp_f32_e32 v189, v189\n"
    "v_cvt_pk_bf16_f32 v178, v180, v181\n"
    "v_mfma_f32_16x16x32_bf16 v[84:87], a[168:171], v[160:163], v[84:87]\n"
    "v_exp_f32_e32 v190, v190\n"
    "v_mfma_f32_16x16x32_bf16 v[84:87], a[172:175], v[168:171], v[84:87]\n"
    "v_exp_f32_e32 v191, v191\n"
    "v_mfma_f32_16x16x32_bf16 v[88:91], a[176:179], v[160:163], v[88:91]\n"
    "v_cvt_pk_bf16_f32 v179, v182, v183\n"
    "v_cvt_pk_bf16_f32 v184, v184, v185\n"
    "v_mfma_f32_16x16x32_bf16 v[88:91], a[180:183], v[168:171], v[88:91]\n"
    "v_cvt_pk_bf16_f32 v185, v186, v187\n"
    "v_cvt_pk_bf16_f32 v186, v188, v189\n"
    "v_mfma_f32_16x16x32_bf16 v[92:95], a[184:187], v[160:163], v[92:95]\n"
    "v_cvt_pk_bf16_f32 v187, v190, v191\n"
    "v_fma_f32 v129, v129, s65, v208\n"
    "v_mfma_f32_16x16x32_bf16 v[92:95], a[188:191], v[168:171], v[92:95]\n"
    "s_cmp_eq_u32 s62, 1\n"
    "s_cbranch_scc1 .Lattn_fin%=\n"
    "v_fma_f32 v130, v130, s65, v208\n"
    "v_fma_f32 v131, v131, s65, v208\n"
    "v_mfma_f32_16x16x32_bf16 v[144:147], a[64:67], a[16:19], 0\n"
    "v_fma_f32 v132, v132, s65, v208\n"
    "v_fma_f32 v133, v133, s65, v208\n"
    "v_mfma_f32_16x16x32_bf16 v[144:147], a[68:71], a[20:23], v[144:147]\n"
    "buffer_load_dwordx4 %[vo3], s[48:51], s57 offen offset:2048 nt lds\n"
    "v_fma_f32 v134, v134, s65, v208\n"
    "v_fma_f32 v135, v135, s65, v208\n"
    "v_fma_f32 v136, v136, s65, v208\n"
    "v_fma_f32 v137, v137, s65, v208\n"
    "v_fma_f32 v138, v138, s65, v208\n"
    "v_mfma_f32_16x16x32_bf16 v[144:147], a[72:75], a[24:27], v[144:147]\n"
    "v_fma_f32 v139, v139, s65, v208\n"
    "v_fma_f32 v140, v140, s65, v208\n"
    "v_fma_f32 v141, v141, s65, v208\n"
    "v_fma_f32 v142, v142, s65, v208\n"
    "v_fma_f32 v143, v143, s65, v208\n"
    "v_accvgpr_mov_b32 a0, a192\n"
    "v_mfma_f32_16x16x32_bf16 v[144:147], a[76:79], a[28:31], v[144:147]\n"
    "v_exp_f32_e32 v128, v128\n"
    "v_exp_f32_e32 v129, v129\n"
    "v_exp_f32_e32 v130, v130\n"
    "v_mfma_f32_16x16x32_bf16 v[148:151], a[80:83], a[16:19], 0\n"
    "v_exp_f32_e32 v131, v131\n"
    "v_exp_f32_e32 v132, v132\n"
    "v_exp_f32_e32 v133, v133\n"
    "v_mfma_f32_16x16x32_bf16 v[148:151], a[84:87], a[20:23], v[148:151]\n"
    "v_exp_f32_e32 v134, v134\n"
    "v_exp_f32_e32 v135, v135\n"
    "v_exp_f32_e32 v136, v136\n"
    "v_mfma_f32_16x16x32_bf16 v[148:151], a[88:91], a[24:27], v[148:151]\n"
    "v_exp_f32_e32 v137, v137\n"
    "v_exp_f32_e32 v138, v138\n"
    "v_mfma_f32_16x16x32_bf16 v[148:151], a[92:95], a[28:31], v[148:151]\n"
    "v_exp_f32_e32 v139, v139\n"
    "v_exp_f32_e32 v140, v140\n"
    "v_mfma_f32_16x16x32_bf16 v[152:155], a[96:99], a[16:19], 0\n"
    "v_exp_f32_e32 v141, v141\n"
    "v_exp_f32_e32 v142, v142\n"
    "v_mfma_f32_16x16x32_bf16 v[152:155], a[100:103], a[20:23], v[152:155]\n"
    "v_exp_f32_e32 v143, v143\n"
    "v_cvt_pk_bf16_f32 v128, v128, v129\n"
    "v_cvt_pk_bf16_f32 v129, v130, v131\n"
    "v_mfma_f32_16x16x32_bf16 v[152:155], a[104:107], a[24:27], v[152:155]\n"
    "v_cvt_pk_bf16_f32 v130, v132, v133\n"
    "v_cvt_pk_bf16_f32 v131, v134, v135\n"
    "v_cvt_pk_bf16_f32 v136, v136, v137\n"
    "v_mfma_f32_16x16x32_bf16 v[152:155], a[108:111], a[28:31], v[152:155]\n"
    "v_cvt_pk_bf16_f32 v137, v138, v139\n"
    "v_cvt_pk_bf16_f32 v138, v140, v141\n"
    "v_cvt_pk_bf16_f32 v139, v142, v143\n"
    "v_mfma_f32_16x16x32_bf16 v[156:159], a[112:115], a[16:19], 0\n"
    "buffer_load_dwordx4 %[vo1], s[48:51], s57 offen offset:3072 nt lds\n"
    "v_accvgpr_mov_b32 a1, a193\n"
    "v_accvgpr_mov_b32 a2, a194\n"
    "v_mfma_f32_16x16x32_bf16 v[156:159], a[116:119], a[20:23], v[156:159]\n"
    "s_add_u32 s57, s57, 0x4000\n"
    "v_accvgpr_mov_b32 a3, a195\n"
    "v_accvgpr_mov_b32 a4, a196\n"
    "v_accvgpr_mov_b32 a5, a197\n"
    "v_mfma_f32_16x16x32_bf16 v[156:159], a[120:123], a[24:27], v[156:159]\n"
    "v_accvgpr_mov_b32 a6, a198\n"
    "v_accvgpr_mov_b32 a7, a199\n"
    "v_mfma_f32_16x16x32_bf16 v[156:159], a[124:127], a[28:31], v[156:159]\n"
    "v_accvgpr_mov_b32 a8, a200\n"
    "v_accvgpr_mov_b32 a9, a201\n"
    "v_mfma_f32_16x16x32_bf16 a[236:239], a[240:243], v[176:179], a[236:239]\n"
    "v_accvgpr_mov_b32 a10, a202\n"
    "v_accvgpr_mov_b32 a11, a203\n"
    "v_mfma_f32_16x16x32_bf16 a[236:239], a[240:243], v[184:187], a[236:239]\n"
    "v_accvgpr_mov_b32 a12, a204\n"
    "v_accvgpr_mov_b32 a13, a205\n"
    "v_mfma_f32_16x16x32_bf16 v[96:99], a[128:131], v[176:179], v[96:99]\n"
    "ds_read_b64_tr_b16 a[128:129], %[vb0] offset:49152\n"
    "v_accvgpr_mov_b32 a14, a206\n"
    "v_mfma_f32_16x16x32_bf16 v[96:99], a[132:135], v[184:187], v[96:99]\n"
    "ds_read_b64_tr_b16 a[130:131], %[vb0] offset:53248\n"
    "v_accvgpr_mov_b32 a15, a207\n"
    "v_mfma_f32_16x16x32_bf16 v[100:103], a[136:139], v[176:179], v[100:103]\n"
    "ds_read_b64_tr_b16 a[132:133], %[vb0] offset:57344\n"
    "v_accvgpr_mov_b32 a16, a208\n"
    "v_mfma_f32_16x16x32_bf16 v[100:103], a[140:143], v[184:187], v[100:103]\n"
    "ds_read_b64_tr_b16 a[134:135], %[vb0] offset:61440\n"
    "v_accvgpr_mov_b32 a17, a209\n"
    "v_mfma_f32_16x16x32_bf16 v[104:107], a[144:147], v[176:179], v[104:107]\n"
    "s_add_u32 m0, s60, 0x8000\n"
    "ds_read_b64_tr_b16 a[136:137], %[vb1] offset:49152\n"
    "v_accvgpr_mov_b32 a18, a210\n"
    "v_mfma_f32_16x16x32_bf16 v[104:107], a[148:151], v[184:187], v[104:107]\n"
    "buffer_load_dwordx4 %[vo0], s[44:47], s56 offen nt lds\n"
    "v_accvgpr_mov_b32 a19, a211\n"
    "v_mfma_f32_16x16x32_bf16 v[108:111], a[152:155], v[176:179], v[108:111]\n"
    "ds_read_b64_tr_b16 a[138:139], %[vb1] offset:53248\n"
    "v_accvgpr_mov_b32 a20, a212\n"
    "v_mfma_f32_16x16x32_bf16 v[108:111], a[156:159], v[184:187], v[108:111]\n"
    "ds_read_b64_tr_b16 a[140:141], %[vb1] offset:57344\n"
    "v_accvgpr_mov_b32 a21, a213\n"
    "v_mfma_f32_16x16x32_bf16 v[112:115], a[160:163], v[176:179], v[112:115]\n"
    "ds_read_b64_tr_b16 a[142:143], %[vb1] offset:61440\n"
    "v_accvgpr_mov_b32 a22, a214\n"
    "v_mfma_f32_16x16x32_bf16 v[112:115], a[164:167], v[184:187], v[112:115]\n"
    "ds_read_b64_tr_b16 a[144:145], %[vb0] offset:49408\n"
    "v_accvgpr_mov_b32 a23, a215\n"
    "v_mfma_f32_16x16x32_bf16 v[116:119], a[168:171], v[176:179], v[116:119]\n"
    "ds_read_b64_tr_b16 a[146:147], %[vb0] offset:53504\n"
    "v_accvgpr_mov_b32 a24, a216\n"
    "v_mfma_f32_16x16x32_bf16 v[116:119], a[172:175], v[184:187], v[116:119]\n"
    "ds_read_b64_tr_b16 a[148:149], %[vb0] offset:57600\n"
    "v_accvgpr_mov_b32 a25, a217\n"
    "v_mfma_f32_16x16x32_bf16 v[120:123], a[176:179], v[176:179], v[120:123]\n"
    "ds_read_b64_tr_b16 a[150:151], %[vb0] offset:61696\n"
    "v_accvgpr_mov_b32 a26, a218\n"
    "v_mfma_f32_16x16x32_bf16 v[120:123], a[180:183], v[184:187], v[120:123]\n"
    "ds_read_b64_tr_b16 a[152:153], %[vb1] offset:49408\n"
    "v_accvgpr_mov_b32 a27, a219\n"
    "v_mfma_f32_16x16x32_bf16 v[124:127], a[184:187], v[176:179], v[124:127]\n"
    "ds_read_b64_tr_b16 a[154:155], %[vb1] offset:53504\n"
    "v_accvgpr_mov_b32 a28, a220\n"
    "v_mfma_f32_16x16x32_bf16 v[124:127], a[188:191], v[184:187], v[124:127]\n"
    "ds_read_b64_tr_b16 a[156:157], %[vb1] offset:57600\n"
    "v_accvgpr_mov_b32 a29, a221\n"
    "v_mfma_f32_16x16x32_bf16 v[160:163], a[64:67], a[32:35], 0\n"
    "buffer_load_dwordx4 %[vo2], s[44:47], s56 offen offset:1024 nt lds\n"
    "v_accvgpr_mov_b32 a30, a222\n"
    "v_mfma_f32_16x16x32_bf16 v[160:163], a[68:71], a[36:39], v[160:163]\n"
    "ds_read_b64_tr_b16 a[158:159], %[vb1] offset:61696\n"
    "v_accvgpr_mov_b32 a31, a223\n"
    "v_mfma_f32_16x16x32_bf16 v[160:163], a[72:75], a[40:43], v[160:163]\n"
    "ds_read_b64_tr_b16 a[160:161], %[vb0] offset:49664\n"
    "v_fma_f32 v144, v144, s65, v209\n"
    "v_mfma_f32_16x16x32_bf16 v[160:163], a[76:79], a[44:47], v[160:163]\n"
    "ds_read_b64_tr_b16 a[162:163], %[vb0] offset:53760\n"
    "v_fma_f32 v145, v145, s65, v209\n"
    "v_fma_f32 v146, v146, s65, v209\n"
    "v_fma_f32 v147, v147, s65, v209\n"
    "v_fma_f32 v148, v148, s65, v209\n"
    "v_fma_f32 v149, v149, s65, v209\n"
    "v_mfma_f32_16x16x32_bf16 v[164:167], a[80:83], a[32:35], 0\n"
    "ds_read_b64_tr_b16 a[164:165], %[vb0] offset:57856\n"
    "v_fma_f32 v150, v150, s65, v209\n"
    "v_fma_f32 v151, v151, s65, v209\n"
    "v_fma_f32 v152, v152, s65, v209\n"
    "v_fma_f32 v153, v153, s65, v209\n"
    "v_fma_f32 v154, v154, s65, v209\n"
    "v_mfma_f32_16x16x32_bf16 v[164:167], a[84:87], a[36:39], v[164:167]\n"
    "ds_read_b64_tr_b16 a[166:167], %[vb0] offset:61952\n"
    "v_fma_f32 v155, v155, s65, v209\n"
    "v_fma_f32 v156, v156, s65, v209\n"
    "v_fma_f32 v157, v157, s65, v209\n"
    "v_fma_f32 v158, v158, s65, v209\n"
    "v_fma_f32 v159, v159, s65, v209\n"
    "v_mfma_f32_16x16x32_bf16 v[164:167], a[88:91], a[40:43], v[164:167]\n"
    "ds_read_b64_tr_b16 a[168:169], %[vb1] offset:49664\n"
    "v_exp_f32_e32 v144, v144\n"
    "v_exp_f32_e32 v145, v145\n"
    "v_mfma_f32_16x16x32_bf16 v[164:167], a[92:95], a[44:47], v[164:167]\n"
    "ds_read_b64_tr_b16 a[170:171], %[vb1] offset:53760\n"
    "v_exp_f32_e32 v146, v146\n"
    "v_exp_f32_e32 v147, v147\n"
    "v_mfma_f32_16x16x32_bf16 v[168:171], a[96:99], a[32:35], 0\n"
    "ds_read_b64_tr_b16 a[172:173], %[vb1] offset:57856\n"
    "v_exp_f32_e32 v148, v148\n"
    "v_exp_f32_e32 v149, v149\n"
    "v_cvt_pk_bf16_f32 v144, v144, v145\n"
    "v_mfma_f32_16x16x32_bf16 v[168:171], a[100:103], a[36:39], v[168:171]\n"
    "ds_read_b64_tr_b16 a[174:175], %[vb1] offset:61952\n"
    "v_exp_f32_e32 v150, v150\n"
    "v_exp_f32_e32 v151, v151\n"
    "v_cvt_pk_bf16_f32 v145, v146, v147\n"
    "v_mfma_f32_16x16x32_bf16 v[168:171], a[104:107], a[40:43], v[168:171]\n"
    "ds_read_b64_tr_b16 a[176:177], %[vb0] offset:49920\n"
    "v_exp_f32_e32 v152, v152\n"
    "v_exp_f32_e32 v153, v153\n"
    "v_cvt_pk_bf16_f32 v146, v148, v149\n"
    "v_mfma_f32_16x16x32_bf16 v[168:171], a[108:111], a[44:47], v[168:171]\n"
    "s_waitcnt vmcnt(13)\n"
    "buffer_load_dwordx4 %[vo3], s[44:47], s56 offen offset:2048 nt lds\n"
    "v_exp_f32_e32 v154, v154\n"
    "v_exp_f32_e32 v155, v155\n"
    "v_cvt_pk_bf16_f32 v147, v150, v151\n"
    "v_mfma_f32_16x16x32_bf16 v[172:175], a[112:115], a[32:35], 0\n"
    "ds_read_b64_tr_b16 a[178:179], %[vb0] offset:54016\n"
    "v_exp_f32_e32 v156, v156\n"
    "v_cvt_pk_bf16_f32 v152, v152, v153\n"
    "v_mfma_f32_16x16x32_bf16 v[172:175], a[116:119], a[36:39], v[172:175]\n"
    "ds_read_b64_tr_b16 a[180:181], %[vb0] offset:58112\n"
    "v_exp_f32_e32 v157, v157\n"
    "v_cvt_pk_bf16_f32 v153, v154, v155\n"
    "v_mfma_f32_16x16x32_bf16 v[172:175], a[120:123], a[40:43], v[172:175]\n"
    "ds_read_b64_tr_b16 a[182:183], %[vb0] offset:62208\n"
    "v_exp_f32_e32 v158, v158\n"
    "v_fma_f32 v160, v160, s65, v210\n"
    "v_mfma_f32_16x16x32_bf16 v[172:175], a[124:127], a[44:47], v[172:175]\n"
    "ds_read_b64_tr_b16 a[184:185], %[vb1] offset:49920\n"
    "v_exp_f32_e32 v159, v159\n"
    "v_cvt_pk_bf16_f32 v154, v156, v157\n"
    "v_mfma_f32_16x16x32_bf16 a[224:227], a[240:243], v[128:131], a[224:227]\n"
    "ds_read_b64_tr_b16 a[186:187], %[vb1] offset:54016\n"
    "v_fma_f32 v161, v161, s65, v210\n"
    "v_fma_f32 v162, v162, s65, v210\n"
    "v_mfma_f32_16x16x32_bf16 a[224:227], a[240:243], v[136:139], a[224:227]\n"
    "s_waitcnt lgkmcnt(10)\n"
    "ds_read_b64_tr_b16 a[188:189], %[vb1] offset:58112\n"
    "v_cvt_pk_bf16_f32 v155, v158, v159\n"
    "v_fma_f32 v163, v163, s65, v210\n"
    "v_mfma_f32_16x16x32_bf16 v[0:3], a[128:131], v[128:131], v[0:3]\n"
    "ds_read_b64_tr_b16 a[190:191], %[vb1] offset:62208\n"
    "v_fma_f32 v164, v164, s65, v210\n"
    "v_fma_f32 v165, v165, s65, v210\n"
    "v_mfma_f32_16x16x32_bf16 v[0:3], a[132:135], v[136:139], v[0:3]\n"
    "ds_read_b128 a[32:35], %[qb] offset:0\n"
    "v_fma_f32 v166, v166, s65, v210\n"
    "v_fma_f32 v167, v167, s65, v210\n"
    "v_mfma_f32_16x16x32_bf16 v[4:7], a[136:139], v[128:131], v[4:7]\n"
    "ds_read_b128 a[36:39], %[qb] offset:256\n"
    "v_fma_f32 v168, v168, s65, v210\n"
    "v_mfma_f32_16x16x32_bf16 v[4:7], a[140:143], v[136:139], v[4:7]\n"
    "ds_read_b128 a[40:43], %[qb] offset:512\n"
    "v_fma_f32 v169, v169, s65, v210\n"
    "v_mfma_f32_16x16x32_bf16 v[8:11], a[144:147], v[128:131], v[8:11]\n"
    "buffer_load_dwordx4 %[vo1], s[44:47], s56 offen offset:3072 nt lds\n"
    "v_accvgpr_read_b32 v220, a224\n"
    "v_mfma_f32_16x16x32_bf16 v[8:11], a[148:151], v[136:139], v[8:11]\n"
    "s_add_u32 s56, s56, 0x4000\n"
    "ds_read_b128 a[44:47], %[qb] offset:768\n"
    "v_mul_f32_e32 v221, s66, v220\n"
    "v_mfma_f32_16x16x32_bf16 v[12:15], a[152:155], v[128:131], v[12:15]\n"
    "v_rcp_f32_e32 v212, v220\n"
    "v_mfma_f32_16x16x32_bf16 v[12:15], a[156:159], v[136:139], v[12:15]\n"
    "v_fma_f32 v170, v170, s65, v210\n"
    "v_fma_f32 v171, v171, s65, v210\n"
    "v_mfma_f32_16x16x32_bf16 v[16:19], a[160:163], v[128:131], v[16:19]\n"
    "v_fma_f32 v172, v172, s65, v210\n"
    "v_fma_f32 v173, v173, s65, v210\n"
    "v_mfma_f32_16x16x32_bf16 v[16:19], a[164:167], v[136:139], v[16:19]\n"
    "s_waitcnt lgkmcnt(7)\n"
    "v_fma_f32 v174, v174, s65, v210\n"
    "v_fma_f32 v175, v175, s65, v210\n"
    "v_mfma_f32_16x16x32_bf16 v[20:23], a[168:171], v[128:131], v[20:23]\n"
    "v_mul_f32_e32 v0, v212, v0\n"
    "v_mul_f32_e32 v1, v212, v1\n"
    "v_mfma_f32_16x16x32_bf16 v[20:23], a[172:175], v[136:139], v[20:23]\n"
    "v_mul_f32_e32 v2, v212, v2\n"
    "v_mul_f32_e32 v3, v212, v3\n"
    "v_mfma_f32_16x16x32_bf16 v[24:27], a[176:179], v[128:131], v[24:27]\n"
    "v_mul_f32_e32 v4, v212, v4\n"
    "v_mul_f32_e32 v5, v212, v5\n"
    "v_mfma_f32_16x16x32_bf16 v[24:27], a[180:183], v[136:139], v[24:27]\n"
    "s_waitcnt lgkmcnt(3)\n"
    "s_add_u32 m0, s60, 0x18000\n"
    "v_mul_f32_e32 v6, v212, v6\n"
    "v_mul_f32_e32 v7, v212, v7\n"
    "v_mfma_f32_16x16x32_bf16 v[28:31], a[184:187], v[128:131], v[28:31]\n"
    "buffer_load_dwordx4 %[vo0], s[48:51], s57 offen nt lds\n"
    "v_cvt_pk_bf16_f32 v192, v0, v1\n"
    "v_mfma_f32_16x16x32_bf16 v[28:31], a[188:191], v[136:139], v[28:31]\n"
    "v_cvt_pk_bf16_f32 v193, v2, v3\n"
    "v_cvt_pk_bf16_f32 v194, v4, v5\n"
    "s_waitcnt vmcnt(13)\n"
    ".Lpace8_%=:\n"
    "s_memrealtime s[74:75]\n"
    "s_waitcnt lgkmcnt(0)\n"
    "s_sub_u32 s74, s74, s36\n"
    "s_cmp_lt_i32 s74, s76\n"
    "s_cbranch_scc1 .Lpace8_%=\n"
    "s_barrier\n"
    "v_mfma_f32_16x16x32_bf16 v[176:179], a[64:67], a[48:51], 0\n"
    "s_cmp_eq_u32 s62, 2\n"
    "s_cselect_b32 s84, s94, s84\n"
    "s_cselect_b32 s85, 150, s85\n"
    "ds_read_b128 a[64:67], %[kb] offset:0\n"
    "v_cvt_pk_bf16_f32 v195, v6, v7\n"
    "v_mfma_f32_16x16x32_bf16 v[176:179], a[68:71], a[52:55], v[176:179]\n"
    "s_sub_i32 s77, s84, s74\n"
    "ds_write_b64 %[ow0], v[192:193] offset:0\n"
    "v_mfma_f32_16x16x32_bf16 v[176:179], a[72:75], a[56:59], v[176:179]\n"
    "s_mul_i32 s77, s77, s39\n"
    "ds_write_b64 %[ow1], v[194:195] offset:0\n"
    "v_mfma_f32_16x16x32_bf16 v[176:179], a[76:79], a[60:63], v[176:179]\n"
    "s_ashr_i32 s77, s77, 16\n"
    "v_exp_f32_e32 v160, v160\n"
    "v_mfma_f32_16x16x32_bf16 v[180:183], a[80:83], a[48:51], 0\n"
    "s_max_i32 s77, s77, s85\n"
    "v_exp_f32_e32 v161, v161\n"
    "v_mfma_f32_16x16x32_bf16 v[180:183], a[84:87], a[52:55], v[180:183]\n"
    "s_min_i32 s77, s77, 200\n"
    "v_exp_f32_e32 v162, v162\n"
    "v_mfma_f32_16x16x32_bf16 v[180:183], a[88:91], a[56:59], v[180:183]\n"
    "s_mov_b32 s71, s77\n"
    "ds_read_b128 a[68:71], %[kb] offset:256\n"
    "v_cvt_pk_bf16_f32 v160, v160, v161\n"
    "v_mfma_f32_16x16x32_bf16 v[180:183], a[92:95], a[60:63], v[180:183]\n"
    "s_mov_b32 s38, s74\n"
    "v_exp_f32_e32 v163, v163\n"
    "v_mfma_f32_16x16x32_bf16 v[184:187], a[96:99], a[48:51], 0\n"
    "s_mov_b32 s39, s37\n"
    "ds_read_b128 a[72:75], %[kb] offset:512\n"
    "v_fma_f32 v176, v176, s65, v211\n"
    "v_mfma_f32_16x16x32_bf16 v[184:187], a[100:103], a[52:55], v[184:187]\n"
    "s_mov_b32 s37, 21845\n"
    "buffer_load_dwordx4 %[vo2], s[48:51], s57 offen offset:1024 nt lds\n"
    "v_mul_f32_e32 v8, v212, v8\n"
    "v_mfma_f32_16x16x32_bf16 v[184:187], a[104:107], a[56:59], v[184:187]\n"
    "s_sub_i32 s74, s74, 4\n"
    "v_mul_f32_e32 v9, v212, v9\n"
    "v_mul_f32_e32 v10, v212, v10\n"
    "v_mfma_f32_16x16x32_bf16 v[184:187], a[108:111], a[60:63], v[184:187]\n"
    "s_max_i32 s76, s74, s38\n"
    "v_mul_f32_e32 v11, v212, v11\n"
    "v_mul_f32_e32 v12, v212, v12\n"
    "v_mfma_f32_16x16x32_bf16 v[188:191], a[112:115], a[48:51], 0\n"
    "s_add_i32 s75, s77, 24\n"
    "ds_read_b128 a[48:51], %[qb] offset:4096\n"
    "v_mul_f32_e32 v13, v212, v13\n"
    "v_mfma_f32_16x16x32_bf16 v[188:191], a[116:119], a[52:55], v[188:191]\n"
    "s_add_u32 s76, s76, s75\n"
    "ds_read_b128 a[52:55], %[qb] offset:4352\n"
    "v_mul_f32_e32 v14, v212, v14\n"
    "v_mfma_f32_16x16x32_bf16 v[188:191], a[120:123], a[56:59], v[188:191]\n"
    "s_add_u32 s38, s38, s75\n"
    "ds_read_b128 a[56:59], %[qb] offset:4608\n"
    "v_mul_f32_e32 v15, v212, v15\n"
    "v_mfma_f32_16x16x32_bf16 v[188:191], a[124:127], a[60:63], v[188:191]\n"
    "s_sub_i32 s77, s77, 16\n"
    "ds_read_b128 a[60:63], %[qb] offset:4864\n"
    "v_cvt_pk_bf16_f32 v196, v8, v9\n"
    "v_cvt_pk_bf16_f32 v197, v10, v11\n"
    "v_mfma_f32_16x16x32_bf16 a[244:247], a[240:243], v[192:195], 0\n"
    "s_max_i32 s77, s77, s71\n"
    "v_cvt_pk_bf16_f32 v198, v12, v13\n"
    "v_cvt_pk_bf16_f32 v199, v14, v15\n"
    "v_mfma_f32_16x16x32_bf16 a[228:231], a[240:243], v[144:147], a[228:231]\n"
    "ds_read_b128 a[76:79], %[kb] offset:768\n"
    "v_cvt_pk_bf16_f32 v161, v162, v163\n"
    "v_mfma_f32_16x16x32_bf16 a[228:231], a[240:243], v[152:155], a[228:231]\n"
    "ds_read_b128 a[80:83], %[kb] offset:4096\n"
    "v_fma_f32 v177, v177, s65, v211\n"
    "v_mfma_f32_16x16x32_bf16 v[32:35], a[128:131], v[144:147], v[32:35]\n"
    "ds_read_b128 a[84:87], %[kb] offset:4352\n"
    "v_exp_f32_e32 v164, v164\n"
    "v_mfma_f32_16x16x32_bf16 v[32:35], a[132:135], v[152:155], v[32:35]\n"
    "buffer_load_dwordx4 %[vo3], s[48:51], s57 offen offset:2048 nt lds\n"
    "v_fma_f32 v178, v178, s65, v211\n"
    "v_mfma_f32_16x16x32_bf16 v[36:39], a[136:139], v[144:147], v[36:39]\n"
    "ds_read_b128 a[88:91], %[kb] offset:4608\n"
    "v_exp_f32_e32 v165, v165\n"
    "v_mfma_f32_16x16x32_bf16 v[36:39], a[140:143], v[152:155], v[36:39]\n"
    "ds_read_b128 a[92:95], %[kb] offset:4864\n"
    "v_fma_f32 v179, v179, s65, v211\n"
    "v_mfma_f32_16x16x32_bf16 v[40:43], a[144:147], v[144:147], v[40:43]\n"
    "ds_read_b128 a[96:99], %[kb] offset:8192\n"
    "v_accvgpr_read_b32 v220, a228\n"
    "v_cvt_pk_bf16_f32 v162, v164, v165\n"
    "v_mfma_f32_16x16x32_bf16 v[40:43], a[148:151], v[152:155], v[40:43]\n"
    "v_rcp_f32_e32 v213, v220\n"
    "v_mfma_f32_16x16x32_bf16 v[44:47], a[152:155], v[144:147], v[44:47]\n"
    "ds_read_b128 a[100:103], %[kb] offset:8448\n"
    "v_fmac_f32_e32 v221, s66, v220\n"
    "v_mfma_f32_16x16x32_bf16 v[44:47], a[156:159], v[152:155], v[44:47]\n"
    "ds_read_b128 a[104:107], %[kb] offset:8704\n"
    "v_fma_f32 v180, v180, s65, v211\n"
    "v_mfma_f32_16x16x32_bf16 v[48:51], a[160:163], v[144:147], v[48:51]\n"
    "ds_write_b64 %[ow2], v[196:197] offset:0\n"
    "v_fma_f32 v181, v181, s65, v211\n"
    "v_mfma_f32_16x16x32_bf16 v[48:51], a[164:167], v[152:155], v[48:51]\n"
    "ds_read_b128 a[108:111], %[kb] offset:8960\n"
    "v_exp_f32_e32 v166, v166\n"
    "v_mfma_f32_16x16x32_bf16 v[52:55], a[168:171], v[144:147], v[52:55]\n"
    "ds_write_b64 %[ow3], v[198:199] offset:0\n"
    "v_mfma_f32_16x16x32_bf16 v[52:55], a[172:175], v[152:155], v[52:55]\n"
    "buffer_load_dwordx4 %[vo1], s[48:51], s57 offen offset:3072 nt lds\n"
    "v_mul_f32_e32 v16, v212, v16\n"
    "v_mfma_f32_16x16x32_bf16 a[244:247], a[240:243], v[196:199], a[244:247]\n"
    "s_add_u32 s57, s57, 0x4000\n"
    "ds_read_b128 a[112:115], %[kb] offset:12288\n"
    "v_mul_f32_e32 v17, v212, v17\n"
    "v_mfma_f32_16x16x32_bf16 v[56:59], a[176:179], v[144:147], v[56:59]\n"
    "ds_read_b128 a[116:119], %[kb] offset:12544\n"
    "v_mul_f32_e32 v18, v212, v18\n"
    "v_mfma_f32_16x16x32_bf16 v[56:59], a[180:183], v[152:155], v[56:59]\n"
    "ds_read_b128 a[120:123], %[kb] offset:12800\n"
    "v_mul_f32_e32 v19, v212, v19\n"
    "v_mfma_f32_16x16x32_bf16 v[60:63], a[184:187], v[144:147], v[60:63]\n"
    "ds_read_b128 a[124:127], %[kb] offset:13056\n"
    "v_mul_f32_e32 v20, v212, v20\n"
    "v_mfma_f32_16x16x32_bf16 v[60:63], a[188:191], v[152:155], v[60:63]\n"
    "v_mul_f32_e32 v21, v212, v21\n"
    "v_mul_f32_e32 v22, v212, v22\n"
    "s_sub_u32 s62, s62, 1\n"
    "s_branch .Lattn_loop%=\n"
    ".Lattn_fin%=:\n"
    "v_mfma_f32_16x16x32_bf16 a[236:239], a[240:243], v[176:179], a[236:239]\n"
    "s_add_u32 s56, s59, 0x1000\n"
    "s_add_u32 s57, s59, 0x2000\n"
    "s_add_u32 s58, s59, 0x3000\n"
    "v_mfma_f32_16x16x32_bf16 a[236:239], a[240:243], v[184:187], a[236:239]\n"
    "v_fma_f32 v130, v130, s65, v208\n"
    "v_fma_f32 v131, v131, s65, v208\n"
    "v_mfma_f32_16x16x32_bf16 v[96:99], a[128:131], v[176:179], v[96:99]\n"
    "ds_read_b64_tr_b16 a[128:129], %[vb0] offset:49152\n"
    "v_fma_f32 v132, v132, s65, v208\n"
    "v_mfma_f32_16x16x32_bf16 v[96:99], a[132:135], v[184:187], v[96:99]\n"
    "ds_read_b64_tr_b16 a[130:131], %[vb0] offset:53248\n"
    "v_fma_f32 v133, v133, s65, v208\n"
    "v_mfma_f32_16x16x32_bf16 v[100:103], a[136:139], v[176:179], v[100:103]\n"
    "ds_read_b64_tr_b16 a[132:133], %[vb0] offset:57344\n"
    "v_fma_f32 v134, v134, s65, v208\n"
    "v_mfma_f32_16x16x32_bf16 v[100:103], a[140:143], v[184:187], v[100:103]\n"
    "ds_read_b64_tr_b16 a[134:135], %[vb0] offset:61440\n"
    "v_fma_f32 v135, v135, s65, v208\n"
    "v_mfma_f32_16x16x32_bf16 v[104:107], a[144:147], v[176:179], v[104:107]\n"
    "ds_read_b64_tr_b16 a[136:137], %[vb1] offset:49152\n"
    "v_fma_f32 v136, v136, s65, v208\n"
    "v_mfma_f32_16x16x32_bf16 v[104:107], a[148:151], v[184:187], v[104:107]\n"
    "ds_read_b64_tr_b16 a[138:139], %[vb1] offset:53248\n"
    "v_fma_f32 v137, v137, s65, v208\n"
    "v_mfma_f32_16x16x32_bf16 v[108:111], a[152:155], v[176:179], v[108:111]\n"
    "ds_read_b64_tr_b16 a[140:141], %[vb1] offset:57344\n"
    "v_fma_f32 v138, v138, s65, v208\n"
    "v_mfma_f32_16x16x32_bf16 v[108:111], a[156:159], v[184:187], v[108:111]\n"
    "ds_read_b64_tr_b16 a[142:143], %[vb1] offset:61440\n"
    "v_fma_f32 v139, v139, s65, v208\n"
    "v_mfma_f32_16x16x32_bf16 v[112:115], a[160:163], v[176:179], v[112:115]\n"
    "ds_read_b64_tr_b16 a[144:145], %[vb0] offset:49408\n"
    "v_fma_f32 v140, v140, s65, v208\n"
    "v_mfma_f32_16x16x32_bf16 v[112:115], a[164:167], v[184:187], v[112:115]\n"
    "ds_read_b64_tr_b16 a[146:147], %[vb0] offset:53504\n"
    "v_fma_f32 v141, v141, s65, v208\n"
    "v_mfma_f32_16x16x32_bf16 v[116:119], a[168:171], v[176:179], v[116:119]\n"
    "ds_read_b64_tr_b16 a[148:149], %[vb0] offset:57600\n"
    "v_fma_f32 v142, v142, s65, v208\n"
    "v_mfma_f32_16x16x32_bf16 v[116:119], a[172:175], v[184:187], v[116:119]\n"
    "ds_read_b64_tr_b16 a[150:151], %[vb0] offset:61696\n"
    "v_fma_f32 v143, v143, s65, v208\n"
    "v_mfma_f32_16x16x32_bf16 v[120:123], a[176:179], v[176:179], v[120:123]\n"
    "ds_read_b64_tr_b16 a[152:153], %[vb1] offset:49408\n"
    "v_exp_f32_e32 v128, v128\n"
    "v_mfma_f32_16x16x32_bf16 v[120:123], a[180:183], v[184:187], v[120:123]\n"
    "ds_read_b64_tr_b16 a[154:155], %[vb1] offset:53504\n"
    "v_exp_f32_e32 v129, v129\n"
    "v_mfma_f32_16x16x32_bf16 v[124:127], a[184:187], v[176:179], v[124:127]\n"
    "ds_read_b64_tr_b16 a[156:157], %[vb1] offset:57600\n"
    "v_exp_f32_e32 v130, v130\n"
    "v_mfma_f32_16x16x32_bf16 v[124:127], a[188:191], v[184:187], v[124:127]\n"
    "ds_read_b64_tr_b16 a[158:159], %[vb1] offset:61696\n"
    "v_exp_f32_e32 v131, v131\n"
    "v_mfma_f32_16x16x32_bf16 v[144:147], a[64:67], a[16:19], 0\n"
    "ds_read_b64_tr_b16 a[160:161], %[vb0] offset:49664\n"
    "v_exp_f32_e32 v132, v132\n"
    "v_mfma_f32_16x16x32_bf16 v[144:147], a[68:71], a[20:23], v[144:147]\n"
    "ds_read_b64_tr_b16 a[162:163], %[vb0] offset:53760\n"
    "v_exp_f32_e32 v133, v133\n"
    "v_mfma_f32_16x16x32_bf16 v[144:147], a[72:75], a[24:27], v[144:147]\n"
    "ds_read_b64_tr_b16 a[164:165], %[vb0] offset:57856\n"
    "v_exp_f32_e32 v134, v134\n"
    "v_mfma_f32_16x16x32_bf16 v[144:147], a[76:79], a[28:31], v[144:147]\n"
    "ds_read_b64_tr_b16 a[166:167], %[vb0] offset:61952\n"
    "v_exp_f32_e32 v135, v135\n"
    "v_mfma_f32_16x16x32_bf16 v[148:151], a[80:83], a[16:19], 0\n"
    "ds_read_b64_tr_b16 a[168:169], %[vb1] offset:49664\n"
    "v_exp_f32_e32 v136, v136\n"
    "v_mfma_f32_16x16x32_bf16 v[148:151], a[84:87], a[20:23], v[148:151]\n"
    "ds_read_b64_tr_b16 a[170:171], %[vb1] offset:53760\n"
    "v_exp_f32_e32 v137, v137\n"
    "v_mfma_f32_16x16x32_bf16 v[148:151], a[88:91], a[24:27], v[148:151]\n"
    "ds_read_b64_tr_b16 a[172:173], %[vb1] offset:57856\n"
    "v_exp_f32_e32 v138, v138\n"
    "v_mfma_f32_16x16x32_bf16 v[148:151], a[92:95], a[28:31], v[148:151]\n"
    "ds_read_b64_tr_b16 a[174:175], %[vb1] offset:61952\n"
    "v_exp_f32_e32 v139, v139\n"
    "v_mfma_f32_16x16x32_bf16 v[152:155], a[96:99], a[16:19], 0\n"
    "ds_read_b64_tr_b16 a[176:177], %[vb0] offset:49920\n"
    "v_exp_f32_e32 v140, v140\n"
    "v_mfma_f32_16x16x32_bf16 v[152:155], a[100:103], a[20:23], v[152:155]\n"
    "ds_read_b64_tr_b16 a[178:179], %[vb0] offset:54016\n"
    "v_exp_f32_e32 v141, v141\n"
    "v_mfma_f32_16x16x32_bf16 v[152:155], a[104:107], a[24:27], v[152:155]\n"
    "ds_read_b64_tr_b16 a[180:181], %[vb0] offset:58112\n"
    "v_exp_f32_e32 v142, v142\n"
    "v_mfma_f32_16x16x32_bf16 v[152:155], a[108:111], a[28:31], v[152:155]\n"
    "ds_read_b64_tr_b16 a[182:183], %[vb0] offset:62208\n"
    "v_exp_f32_e32 v143, v143\n"
    "v_mfma_f32_16x16x32_bf16 v[156:159], a[112:115], a[16:19], 0\n"
    "ds_read_b64_tr_b16 a[184:185], %[vb1] offset:49920\n"
    "v_cvt_pk_bf16_f32 v128, v128, v129\n"
    "v_mfma_f32_16x16x32_bf16 v[156:159], a[116:119], a[20:23], v[156:159]\n"
    "ds_read_b64_tr_b16 a[186:187], %[vb1] offset:54016\n"
    "v_cvt_pk_bf16_f32 v129, v130, v131\n"
    "v_mfma_f32_16x16x32_bf16 v[156:159], a[120:123], a[24:27], v[156:159]\n"
    "ds_read_b64_tr_b16 a[188:189], %[vb1] offset:58112\n"
    "v_cvt_pk_bf16_f32 v130, v132, v133\n"
    "v_mfma_f32_16x16x32_bf16 v[156:159], a[124:127], a[28:31], v[156:159]\n"
    "ds_read_b64_tr_b16 a[190:191], %[vb1] offset:62208\n"
    "v_cvt_pk_bf16_f32 v131, v134, v135\n"
    "v_cvt_pk_bf16_f32 v136, v136, v137\n"
    "v_cvt_pk_bf16_f32 v137, v138, v139\n"
    "v_mfma_f32_16x16x32_bf16 a[224:227], a[240:243], v[128:131], a[224:227]\n"
    "s_waitcnt lgkmcnt(11)\n"
    "v_cvt_pk_bf16_f32 v138, v140, v141\n"
    "v_cvt_pk_bf16_f32 v139, v142, v143\n"
    "v_mfma_f32_16x16x32_bf16 v[0:3], a[128:131], v[128:131], v[0:3]\n"
    "s_waitcnt vmcnt(9)\n"
    "v_fma_f32 v144, v144, s65, v209\n"
    "v_fma_f32 v145, v145, s65, v209\n"
    "v_mfma_f32_16x16x32_bf16 a[224:227], a[240:243], v[136:139], a[224:227]\n"
    "v_fma_f32 v146, v146, s65, v209\n"
    "v_fma_f32 v147, v147, s65, v209\n"
    "v_mfma_f32_16x16x32_bf16 v[0:3], a[132:135], v[136:139], v[0:3]\n"
    "v_fma_f32 v148, v148, s65, v209\n"
    "v_fma_f32 v149, v149, s65, v209\n"
    "v_mfma_f32_16x16x32_bf16 v[4:7], a[136:139], v[128:131], v[4:7]\n"
    "s_nop 1\n"
    "v_accvgpr_read_b32 v216, a224\n"
    "v_rcp_f32_e32 v212, v216\n"
    "v_mfma_f32_16x16x32_bf16 v[4:7], a[140:143], v[136:139], v[4:7]\n"
    "v_mul_f32_e32 v0, v212, v0\n"
    "v_mul_f32_e32 v1, v212, v1\n"
    "v_mfma_f32_16x16x32_bf16 v[8:11], a[144:147], v[128:131], v[8:11]\n"
    "v_mul_f32_e32 v2, v212, v2\n"
    "v_mul_f32_e32 v3, v212, v3\n"
    "v_mfma_f32_16x16x32_bf16 v[8:11], a[148:151], v[136:139], v[8:11]\n"
    "s_nop 1\n"
    "v_mul_f32_e32 v4, v212, v4\n"
    "v_mul_f32_e32 v5, v212, v5\n"
    "v_mfma_f32_16x16x32_bf16 v[12:15], a[152:155], v[128:131], v[12:15]\n"
    "v_mul_f32_e32 v6, v212, v6\n"
    "v_mul_f32_e32 v7, v212, v7\n"
    "v_mfma_f32_16x16x32_bf16 v[12:15], a[156:159], v[136:139], v[12:15]\n"
    "v_cvt_pk_bf16_f32 v0, v0, v1\n"
    "v_cvt_pk_bf16_f32 v1, v2, v3\n"
    "v_mfma_f32_16x16x32_bf16 v[16:19], a[160:163], v[128:131], v[16:19]\n"
    "ds_write_b64 %[ow0], v[0:1] offset:0\n"
    "v_mfma_f32_16x16x32_bf16 v[16:19], a[164:167], v[136:139], v[16:19]\n"
    "s_waitcnt lgkmcnt(1)\n"
    "v_cvt_pk_bf16_f32 v2, v4, v5\n"
    "v_cvt_pk_bf16_f32 v3, v6, v7\n"
    "v_mfma_f32_16x16x32_bf16 v[20:23], a[168:171], v[128:131], v[20:23]\n"
    "ds_write_b64 %[ow1], v[2:3] offset:0\n"
    "v_mfma_f32_16x16x32_bf16 v[20:23], a[172:175], v[136:139], v[20:23]\n"
    "v_mul_f32_e32 v8, v212, v8\n"
    "v_mul_f32_e32 v9, v212, v9\n"
    "v_mfma_f32_16x16x32_bf16 v[24:27], a[176:179], v[128:131], v[24:27]\n"
    "v_mul_f32_e32 v10, v212, v10\n"
    "v_mul_f32_e32 v11, v212, v11\n"
    "v_mfma_f32_16x16x32_bf16 v[24:27], a[180:183], v[136:139], v[24:27]\n"
    "v_mul_f32_e32 v12, v212, v12\n"
    "v_mul_f32_e32 v13, v212, v13\n"
    "v_mfma_f32_16x16x32_bf16 v[28:31], a[184:187], v[128:131], v[28:31]\n"
    "v_mul_f32_e32 v14, v212, v14\n"
    "v_mul_f32_e32 v15, v212, v15\n"
    "v_mfma_f32_16x16x32_bf16 v[28:31], a[188:191], v[136:139], v[28:31]\n"
    "v_cvt_pk_bf16_f32 v8, v8, v9\n"
    "v_cvt_pk_bf16_f32 v9, v10, v11\n"
    "v_mfma_f32_16x16x32_bf16 a[244:247], a[240:243], v[0:3], 0\n"
    "ds_write_b64 %[ow2], v[8:9] offset:0\n"
    "v_cvt_pk_bf16_f32 v10, v12, v13\n"
    "v_cvt_pk_bf16_f32 v11, v14, v15\n"
    "v_mul_f32_e32 v16, v212, v16\n"
    "v_mul_f32_e32 v17, v212, v17\n"
    "v_mfma_f32_16x16x32_bf16 a[244:247], a[240:243], v[8:11], a[244:247]\n"
    "ds_write_b64 %[ow3], v[10:11] offset:0\n"
    "v_mul_f32_e32 v18, v212, v18\n"
    "v_mul_f32_e32 v19, v212, v19\n"
    "v_mul_f32_e32 v20, v212, v20\n"
    "v_mul_f32_e32 v21, v212, v21\n"
    "v_mul_f32_e32 v22, v212, v22\n"
    "v_mul_f32_e32 v23, v212, v23\n"
    "v_cvt_pk_bf16_f32 v16, v16, v17\n"
    "v_cvt_pk_bf16_f32 v17, v18, v19\n"
    "v_cvt_pk_bf16_f32 v18, v20, v21\n"
    "v_cvt_pk_bf16_f32 v19, v22, v23\n"
    "v_mul_f32_e32 v24, v212, v24\n"
    "v_mul_f32_e32 v25, v212, v25\n"
    "v_mfma_f32_16x16x32_bf16 a[244:247], a[240:243], v[16:19], a[244:247]\n"
    "ds_write_b64 %[ow0], v[16:17] offset:128\n"
    "ds_write_b64 %[ow1], v[18:19] offset:128\n"
    "v_mul_f32_e32 v26, v212, v26\n"
    "v_mul_f32_e32 v27, v212, v27\n"
    "v_mul_f32_e32 v28, v212, v28\n"
    "v_mul_f32_e32 v29, v212, v29\n"
    "v_mul_f32_e32 v30, v212, v30\n"
    "v_mul_f32_e32 v31, v212, v31\n"
    "v_cvt_pk_bf16_f32 v24, v24, v25\n"
    "v_cvt_pk_bf16_f32 v25, v26, v27\n"
    "v_cvt_pk_bf16_f32 v26, v28, v29\n"
    "v_cvt_pk_bf16_f32 v27, v30, v31\n"
    "v_fma_f32 v150, v150, s65, v209\n"
    "v_fma_f32 v151, v151, s65, v209\n"
    "v_mfma_f32_16x16x32_bf16 a[244:247], a[240:243], v[24:27], a[244:247]\n"
    "ds_write_b64 %[ow2], v[24:25] offset:128\n"
    "v_mfma_f32_16x16x32_bf16 v[160:163], a[64:67], a[32:35], 0\n"
    "ds_write_b64 %[ow3], v[26:27] offset:128\n"
    "v_mfma_f32_16x16x32_bf16 v[160:163], a[68:71], a[36:39], v[160:163]\n"
    "ds_read_b128 a[192:195], %[or0] offset:0\n"
    "v_fma_f32 v152, v152, s65, v209\n"
    "v_mfma_f32_16x16x32_bf16 v[160:163], a[72:75], a[40:43], v[160:163]\n"
    "ds_read_b128 a[196:199], %[or1] offset:1024\n"
    "v_fma_f32 v153, v153, s65, v209\n"
    "v_mfma_f32_16x16x32_bf16 v[160:163], a[76:79], a[44:47], v[160:163]\n"
    "ds_read_b128 a[200:203], %[or0] offset:2048\n"
    "v_fma_f32 v154, v154, s65, v209\n"
    "v_mfma_f32_16x16x32_bf16 v[164:167], a[80:83], a[32:35], 0\n"
    "ds_read_b128 a[204:207], %[or1] offset:3072\n"
    "v_fma_f32 v155, v155, s65, v209\n"
    "v_mfma_f32_16x16x32_bf16 v[164:167], a[84:87], a[36:39], v[164:167]\n"
    "v_fma_f32 v156, v156, s65, v209\n"
    "v_fma_f32 v157, v157, s65, v209\n"
    "v_mfma_f32_16x16x32_bf16 v[164:167], a[88:91], a[40:43], v[164:167]\n"
    "v_fma_f32 v158, v158, s65, v209\n"
    "v_fma_f32 v159, v159, s65, v209\n"
    "v_mfma_f32_16x16x32_bf16 v[164:167], a[92:95], a[44:47], v[164:167]\n"
    "v_exp_f32_e32 v144, v144\n"
    "v_mfma_f32_16x16x32_bf16 v[168:171], a[96:99], a[32:35], 0\n"
    "v_exp_f32_e32 v145, v145\n"
    "v_mfma_f32_16x16x32_bf16 v[168:171], a[100:103], a[36:39], v[168:171]\n"
    "v_exp_f32_e32 v146, v146\n"
    "v_mfma_f32_16x16x32_bf16 v[168:171], a[104:107], a[40:43], v[168:171]\n"
    "s_waitcnt lgkmcnt(3)\n"
    "buffer_store_dwordx4 a[192:195], %[ost], s[52:55], s59 offen sc1\n"
    "v_exp_f32_e32 v147, v147\n"
    "v_mfma_f32_16x16x32_bf16 v[168:171], a[108:111], a[44:47], v[168:171]\n"
    "v_exp_f32_e32 v148, v148\n"
    "v_mfma_f32_16x16x32_bf16 v[172:175], a[112:115], a[32:35], 0\n"
    "v_exp_f32_e32 v149, v149\n"
    "v_mfma_f32_16x16x32_bf16 v[172:175], a[116:119], a[36:39], v[172:175]\n"
    "v_cvt_pk_bf16_f32 v144, v144, v145\n"
    "v_exp_f32_e32 v150, v150\n"
    "v_mfma_f32_16x16x32_bf16 v[172:175], a[120:123], a[40:43], v[172:175]\n"
    "s_waitcnt lgkmcnt(2)\n"
    "buffer_store_dwordx4 a[196:199], %[ost], s[52:55], s59 offen offset:1024 sc1\n"
    "v_exp_f32_e32 v151, v151\n"
    "v_mfma_f32_16x16x32_bf16 v[172:175], a[124:127], a[44:47], v[172:175]\n"
    "v_cvt_pk_bf16_f32 v145, v146, v147\n"
    "v_exp_f32_e32 v152, v152\n"
    "v_exp_f32_e32 v153, v153\n"
    "v_cvt_pk_bf16_f32 v146, v148, v149\n"
    "v_exp_f32_e32 v154, v154\n"
    "v_exp_f32_e32 v155, v155\n"
    "v_cvt_pk_bf16_f32 v147, v150, v151\n"
    "v_exp_f32_e32 v156, v156\n"
    "s_nop 0\n"
    "v_mfma_f32_16x16x32_bf16 a[228:231], a[240:243], v[144:147], a[228:231]\n"
    "s_waitcnt lgkmcnt(0)\n"
    "buffer_store_dwordx4 a[200:203], %[ost], s[52:55], s59 offen offset:2048 sc1\n"
    "v_cvt_pk_bf16_f32 v152, v152, v153\n"
    "v_mfma_f32_16x16x32_bf16 v[32:35], a[128:131], v[144:147], v[32:35]\n"
    "v_exp_f32_e32 v157, v157\n"
    "v_mfma_f32_16x16x32_bf16 v[36:39], a[136:139], v[144:147], v[36:39]\n"
    "v_cvt_pk_bf16_f32 v153, v154, v155\n"
    "v_exp_f32_e32 v158, v158\n"
    "v_mfma_f32_16x16x32_bf16 v[40:43], a[144:147], v[144:147], v[40:43]\n"
    "v_exp_f32_e32 v159, v159\n"
    "v_mfma_f32_16x16x32_bf16 v[44:47], a[152:155], v[144:147], v[44:47]\n"
    "buffer_store_dwordx4 a[204:207], %[ost], s[52:55], s59 offen offset:3072 sc1\n"
    "v_cvt_pk_bf16_f32 v154, v156, v157\n"
    "v_mfma_f32_16x16x32_bf16 v[48:51], a[160:163], v[144:147], v[48:51]\n"
    "v_cvt_pk_bf16_f32 v155, v158, v159\n"
    "v_fma_f32 v160, v160, s65, v210\n"
    "v_mfma_f32_16x16x32_bf16 v[52:55], a[168:171], v[144:147], v[52:55]\n"
    "v_fma_f32 v161, v161, s65, v210\n"
    "v_fma_f32 v162, v162, s65, v210\n"
    "v_mfma_f32_16x16x32_bf16 a[228:231], a[240:243], v[152:155], a[228:231]\n"
    "v_fma_f32 v163, v163, s65, v210\n"
    "v_fma_f32 v164, v164, s65, v210\n"
    "v_mfma_f32_16x16x32_bf16 v[32:35], a[132:135], v[152:155], v[32:35]\n"
    "v_fma_f32 v165, v165, s65, v210\n"
    "v_fma_f32 v166, v166, s65, v210\n"
    "v_mfma_f32_16x16x32_bf16 v[36:39], a[140:143], v[152:155], v[36:39]\n"
    "s_nop 1\n"
    "v_accvgpr_read_b32 v217, a228\n"
    "v_rcp_f32_e32 v213, v217\n"
    "v_mfma_f32_16x16x32_bf16 v[40:43], a[148:151], v[152:155], v[40:43]\n"
    "v_mul_f32_e32 v32, v213, v32\n"
    "v_mul_f32_e32 v33, v213, v33\n"
    "v_mfma_f32_16x16x32_bf16 v[44:47], a[156:159], v[152:155], v[44:47]\n"
    "v_mul_f32_e32 v34, v213, v34\n"
    "v_mul_f32_e32 v35, v213, v35\n"
    "v_mfma_f32_16x16x32_bf16 v[48:51], a[164:167], v[152:155], v[48:51]\n"
    "v_mul_f32_e32 v36, v213, v36\n"
    "v_mul_f32_e32 v37, v213, v37\n"
    "v_mfma_f32_16x16x32_bf16 v[52:55], a[172:175], v[152:155], v[52:55]\n"
    "v_mul_f32_e32 v38, v213, v38\n"
    "v_mul_f32_e32 v39, v213, v39\n"
    "v_mfma_f32_16x16x32_bf16 v[56:59], a[176:179], v[144:147], v[56:59]\n"
    "v_cvt_pk_bf16_f32 v32, v32, v33\n"
    "v_cvt_pk_bf16_f32 v33, v34, v35\n"
    "v_mfma_f32_16x16x32_bf16 v[56:59], a[180:183], v[152:155], v[56:59]\n"
    "ds_write_b64 %[ow0], v[32:33] offset:4096\n"
    "v_mfma_f32_16x16x32_bf16 v[60:63], a[184:187], v[144:147], v[60:63]\n"
    "v_cvt_pk_bf16_f32 v34, v36, v37\n"
    "v_cvt_pk_bf16_f32 v35, v38, v39\n"
    "v_mfma_f32_16x16x32_bf16 v[60:63], a[188:191], v[152:155], v[60:63]\n"
    "ds_write_b64 %[ow1], v[34:35] offset:4096\n"
    "v_mfma_f32_16x16x32_bf16 a[244:247], a[240:243], v[32:35], a[244:247]\n"
    "v_mul_f32_e32 v40, v213, v40\n"
    "v_mul_f32_e32 v41, v213, v41\n"
    "v_mul_f32_e32 v42, v213, v42\n"
    "v_mul_f32_e32 v43, v213, v43\n"
    "v_mul_f32_e32 v44, v213, v44\n"
    "v_mul_f32_e32 v45, v213, v45\n"
    "v_mul_f32_e32 v46, v213, v46\n"
    "v_mul_f32_e32 v47, v213, v47\n"
    "v_cvt_pk_bf16_f32 v40, v40, v41\n"
    "v_cvt_pk_bf16_f32 v41, v42, v43\n"
    "v_cvt_pk_bf16_f32 v42, v44, v45\n"
    "v_cvt_pk_bf16_f32 v43, v46, v47\n"
    "v_mul_f32_e32 v48, v213, v48\n"
    "v_mul_f32_e32 v49, v213, v49\n"
    "v_mfma_f32_16x16x32_bf16 a[244:247], a[240:243], v[40:43], a[244:247]\n"
    "ds_write_b64 %[ow2], v[40:41] offset:4096\n"
    "ds_write_b64 %[ow3], v[42:43] offset:4096\n"
    "v_mul_f32_e32 v50, v213, v50\n"
    "v_mul_f32_e32 v51, v213, v51\n"
    "v_mul_f32_e32 v52, v213, v52\n"
    "v_mul_f32_e32 v53, v213, v53\n"
    "v_mul_f32_e32 v54, v213, v54\n"
    "v_mul_f32_e32 v55, v213, v55\n"
    "v_cvt_pk_bf16_f32 v48, v48, v49\n"
    "v_cvt_pk_bf16_f32 v49, v50, v51\n"
    "v_cvt_pk_bf16_f32 v50, v52, v53\n"
    "v_cvt_pk_bf16_f32 v51, v54, v55\n"
    "v_mul_f32_e32 v56, v213, v56\n"
    "v_mul_f32_e32 v57, v213, v57\n"
    "v_mfma_f32_16x16x32_bf16 a[244:247], a[240:243], v[48:51], a[244:247]\n"
    "ds_write_b64 %[ow0], v[48:49] offset:4224\n"
    "ds_write_b64 %[ow1], v[50:51] offset:4224\n"
    "v_mul_f32_e32 v58, v213, v58\n"
    "v_mul_f32_e32 v59, v213, v59\n"
    "v_mul_f32_e32 v60, v213, v60\n"
    "v_mul_f32_e32 v61, v213, v61\n"
    "v_mul_f32_e32 v62, v213, v62\n"
    "v_mul_f32_e32 v63, v213, v63\n"
    "v_cvt_pk_bf16_f32 v56, v56, v57\n"
    "v_cvt_pk_bf16_f32 v57, v58, v59\n"
    "v_cvt_pk_bf16_f32 v58, v60, v61\n"
    "v_cvt_pk_bf16_f32 v59, v62, v63\n"
    "v_fma_f32 v167, v167, s65, v210\n"
    "v_fma_f32 v168, v168, s65, v210\n"
    "v_mfma_f32_16x16x32_bf16 a[244:247], a[240:243], v[56:59], a[244:247]\n"
    "ds_write_b64 %[ow2], v[56:57] offset:4224\n"
    "v_mfma_f32_16x16x32_bf16 v[176:179], a[64:67], a[48:51], 0\n"
    "ds_write_b64 %[ow3], v[58:59] offset:4224\n"
    "v_mfma_f32_16x16x32_bf16 v[176:179], a[68:71], a[52:55], v[176:179]\n"
    "ds_read_b128 a[208:211], %[or0] offset:4096\n"
    "v_fma_f32 v169, v169, s65, v210\n"
    "v_mfma_f32_16x16x32_bf16 v[176:179], a[72:75], a[56:59], v[176:179]\n"
    "ds_read_b128 a[212:215], %[or1] offset:5120\n"
    "v_fma_f32 v170, v170, s65, v210\n"
    "v_mfma_f32_16x16x32_bf16 v[176:179], a[76:79], a[60:63], v[176:179]\n"
    "ds_read_b128 a[216:219], %[or0] offset:6144\n"
    "v_fma_f32 v171, v171, s65, v210\n"
    "v_mfma_f32_16x16x32_bf16 v[180:183], a[80:83], a[48:51], 0\n"
    "ds_read_b128 a[220:223], %[or1] offset:7168\n"
    "v_fma_f32 v172, v172, s65, v210\n"
    "v_mfma_f32_16x16x32_bf16 v[180:183], a[84:87], a[52:55], v[180:183]\n"
    "v_fma_f32 v173, v173, s65, v210\n"
    "v_fma_f32 v174, v174, s65, v210\n"
    "v_mfma_f32_16x16x32_bf16 v[180:183], a[88:91], a[56:59], v[180:183]\n"
    "v_fma_f32 v175, v175, s65, v210\n"
    "v_exp_f32_e32 v160, v160\n"
    "v_mfma_f32_16x16x32_bf16 v[180:183], a[92:95], a[60:63], v[180:183]\n"
    "v_exp_f32_e32 v161, v161\n"
    "v_mfma_f32_16x16x32_bf16 v[184:187], a[96:99], a[48:51], 0\n"
    "v_exp_f32_e32 v162, v162\n"
    "v_mfma_f32_16x16x32_bf16 v[184:187], a[100:103], a[52:55], v[184:187]\n"
    "v_cvt_pk_bf16_f32 v160, v160, v161\n"
    "v_exp_f32_e32 v163, v163\n"
    "v_mfma_f32_16x16x32_bf16 v[184:187], a[104:107], a[56:59], v[184:187]\n"
    "s_waitcnt lgkmcnt(3)\n"
    "buffer_store_dwordx4 a[208:211], %[ost], s[52:55], s56 offen sc1\n"
    "v_cvt_pk_bf16_f32 v161, v162, v163\n"
    "v_mfma_f32_16x16x32_bf16 v[184:187], a[108:111], a[60:63], v[184:187]\n"
    "v_exp_f32_e32 v164, v164\n"
    "v_mfma_f32_16x16x32_bf16 v[188:191], a[112:115], a[48:51], 0\n"
    "v_exp_f32_e32 v165, v165\n"
    "v_mfma_f32_16x16x32_bf16 v[188:191], a[116:119], a[52:55], v[188:191]\n"
    "v_cvt_pk_bf16_f32 v162, v164, v165\n"
    "v_exp_f32_e32 v166, v166\n"
    "v_mfma_f32_16x16x32_bf16 v[188:191], a[120:123], a[56:59], v[188:191]\n"
    "s_waitcnt lgkmcnt(2)\n"
    "buffer_store_dwordx4 a[212:215], %[ost], s[52:55], s56 offen offset:1024 sc1\n"
    "v_exp_f32_e32 v167, v167\n"
    "v_mfma_f32_16x16x32_bf16 v[188:191], a[124:127], a[60:63], v[188:191]\n"
    "v_exp_f32_e32 v168, v168\n"
    "v_exp_f32_e32 v169, v169\n"
    "v_exp_f32_e32 v170, v170\n"
    "v_exp_f32_e32 v171, v171\n"
    "v_exp_f32_e32 v172, v172\n"
    "v_exp_f32_e32 v173, v173\n"
    "s_waitcnt lgkmcnt(1)\n"
    "buffer_store_dwordx4 a[216:219], %[ost], s[52:55], s56 offen offset:2048 sc1\n"
    "v_exp_f32_e32 v174, v174\n"
    "v_exp_f32_e32 v175, v175\n"
    "v_cvt_pk_bf16_f32 v163, v166, v167\n"
    "v_cvt_pk_bf16_f32 v168, v168, v169\n"
    "v_cvt_pk_bf16_f32 v169, v170, v171\n"
    "v_mfma_f32_16x16x32_bf16 a[232:235], a[240:243], v[160:163], a[232:235]\n"
    "v_cvt_pk_bf16_f32 v170, v172, v173\n"
    "v_cvt_pk_bf16_f32 v171, v174, v175\n"
    "v_mfma_f32_16x16x32_bf16 v[64:67], a[128:131], v[160:163], v[64:67]\n"
    "v_fma_f32 v176, v176, s65, v211\n"
    "v_fma_f32 v177, v177, s65, v211\n"
    "v_mfma_f32_16x16x32_bf16 a[232:235], a[240:243], v[168:171], a[232:235]\n"
    "s_waitcnt lgkmcnt(0)\n"
    "buffer_store_dwordx4 a[220:223], %[ost], s[52:55], s56 offen offset:3072 sc1\n"
    "v_fma_f32 v178, v178, s65, v211\n"
    "v_mfma_f32_16x16x32_bf16 v[64:67], a[132:135], v[168:171], v[64:67]\n"
    "v_fma_f32 v179, v179, s65, v211\n"
    "v_fma_f32 v180, v180, s65, v211\n"
    "v_mfma_f32_16x16x32_bf16 v[68:71], a[136:139], v[160:163], v[68:71]\n"
    "s_nop 0\n"
    "v_accvgpr_read_b32 v218, a232\n"
    "v_rcp_f32_e32 v214, v218\n"
    "v_mfma_f32_16x16x32_bf16 v[68:71], a[140:143], v[168:171], v[68:71]\n"
    "s_nop 0\n"
    "v_mul_f32_e32 v64, v214, v64\n"
    "v_mul_f32_e32 v65, v214, v65\n"
    "v_mfma_f32_16x16x32_bf16 v[72:75], a[144:147], v[160:163], v[72:75]\n"
    "v_mul_f32_e32 v66, v214, v66\n"
    "v_mul_f32_e32 v67, v214, v67\n"
    "v_mfma_f32_16x16x32_bf16 v[72:75], a[148:151], v[168:171], v[72:75]\n"
    "s_nop 0\n"
    "v_mul_f32_e32 v68, v214, v68\n"
    "v_mul_f32_e32 v69, v214, v69\n"
    "v_mfma_f32_16x16x32_bf16 v[76:79], a[152:155], v[160:163], v[76:79]\n"
    "v_mul_f32_e32 v70, v214, v70\n"
    "v_mul_f32_e32 v71, v214, v71\n"
    "v_mfma_f32_16x16x32_bf16 v[76:79], a[156:159], v[168:171], v[76:79]\n"
    "v_cvt_pk_bf16_f32 v64, v64, v65\n"
    "v_cvt_pk_bf16_f32 v65, v66, v67\n"
    "v_mfma_f32_16x16x32_bf16 v[80:83], a[160:163], v[160:163], v[80:83]\n"
    "ds_write_b64 %[ow0], v[64:65] offset:0\n"
    "v_mfma_f32_16x16x32_bf16 v[80:83], a[164:167], v[168:171], v[80:83]\n"
    "v_cvt_pk_bf16_f32 v66, v68, v69\n"
    "v_cvt_pk_bf16_f32 v67, v70, v71\n"
    "v_mfma_f32_16x16x32_bf16 v[84:87], a[168:171], v[160:163], v[84:87]\n"
    "ds_write_b64 %[ow1], v[66:67] offset:0\n"
    "v_mfma_f32_16x16x32_bf16 v[84:87], a[172:175], v[168:171], v[84:87]\n"
    "v_mul_f32_e32 v72, v214, v72\n"
    "v_mul_f32_e32 v73, v214, v73\n"
    "v_mfma_f32_16x16x32_bf16 v[88:91], a[176:179], v[160:163], v[88:91]\n"
    "v_mul_f32_e32 v74, v214, v74\n"
    "v_mul_f32_e32 v75, v214, v75\n"
    "v_mfma_f32_16x16x32_bf16 v[88:91], a[180:183], v[168:171], v[88:91]\n"
    "v_mul_f32_e32 v76, v214, v76\n"
    "v_mul_f32_e32 v77, v214, v77\n"
    "v_mfma_f32_16x16x32_bf16 v[92:95], a[184:187], v[160:163], v[92:95]\n"
    "v_mul_f32_e32 v78, v214, v78\n"
    "v_mul_f32_e32 v79, v214, v79\n"
    "v_mfma_f32_16x16x32_bf16 v[92:95], a[188:191], v[168:171], v[92:95]\n"
    "v_cvt_pk_bf16_f32 v72, v72, v73\n"
    "v_cvt_pk_bf16_f32 v73, v74, v75\n"
    "v_mfma_f32_16x16x32_bf16 a[244:247], a[240:243], v[64:67], a[244:247]\n"
    "ds_write_b64 %[ow2], v[72:73] offset:0\n"
    "v_cvt_pk_bf16_f32 v74, v76, v77\n"
    "v_cvt_pk_bf16_f32 v75, v78, v79\n"
    "v_mul_f32_e32 v80, v214, v80\n"
    "v_mul_f32_e32 v81, v214, v81\n"
    "v_mfma_f32_16x16x32_bf16 a[244:247], a[240:243], v[72:75], a[244:247]\n"
    "ds_write_b64 %[ow3], v[74:75] offset:0\n"
    "v_mul_f32_e32 v82, v214, v82\n"
    "v_mul_f32_e32 v83, v214, v83\n"
    "v_mul_f32_e32 v84, v214, v84\n"
    "v_mul_f32_e32 v85, v214, v85\n"
    "v_mul_f32_e32 v86, v214, v86\n"
    "v_mul_f32_e32 v87, v214, v87\n"
    "v_cvt_pk_bf16_f32 v80, v80, v81\n"
    "v_cvt_pk_bf16_f32 v81, v82, v83\n"
    "v_cvt_pk_bf16_f32 v82, v84, v85\n"
    "v_cvt_pk_bf16_f32 v83, v86, v87\n"
    "v_mul_f32_e32 v88, v214, v88\n"
    "v_mul_f32_e32 v89, v214, v89\n"
    "v_mfma_f32_16x16x32_bf16 a[244:247], a[240:243], v[80:83], a[244:247]\n"
    "ds_write_b64 %[ow0], v[80:81] offset:128\n"
    "ds_write_b64 %[ow1], v[82:83] offset:128\n"
    "v_mul_f32_e32 v90, v214, v90\n"
    "v_mul_f32_e32 v91, v214, v91\n"
    "v_mul_f32_e32 v92, v214, v92\n"
    "v_mul_f32_e32 v93, v214, v93\n"
    "v_mul_f32_e32 v94, v214, v94\n"
    "v_mul_f32_e32 v95, v214, v95\n"
    "v_cvt_pk_bf16_f32 v88, v88, v89\n"
    "v_cvt_pk_bf16_f32 v89, v90, v91\n"
    "v_cvt_pk_bf16_f32 v90, v92, v93\n"
    "v_cvt_pk_bf16_f32 v91, v94, v95\n"
    "v_fma_f32 v181, v181, s65, v211\n"
    "v_fma_f32 v182, v182, s65, v211\n"
    "v_mfma_f32_16x16x32_bf16 a[244:247], a[240:243], v[88:91], a[244:247]\n"
    "ds_write_b64 %[ow2], v[88:89] offset:128\n"
    "ds_write_b64 %[ow3], v[90:91] offset:128\n"
    "ds_read_b128 a[64:67], %[or0] offset:0\n"
    "v_fma_f32 v183, v183, s65, v211\n"
    "v_fma_f32 v184, v184, s65, v211\n"
    "v_fma_f32 v185, v185, s65, v211\n"
    "ds_read_b128 a[68:71], %[or1] offset:1024\n"
    "v_fma_f32 v186, v186, s65, v211\n"
    "v_fma_f32 v187, v187, s65, v211\n"
    "v_fma_f32 v188, v188, s65, v211\n"
    "ds_read_b128 a[72:75], %[or0] offset:2048\n"
    "v_fma_f32 v189, v189, s65, v211\n"
    "v_fma_f32 v190, v190, s65, v211\n"
    "v_fma_f32 v191, v191, s65, v211\n"
    "ds_read_b128 a[76:79], %[or1] offset:3072\n"
    "v_exp_f32_e32 v176, v176\n"
    "v_exp_f32_e32 v177, v177\n"
    "v_exp_f32_e32 v178, v178\n"
    "v_exp_f32_e32 v179, v179\n"
    "v_exp_f32_e32 v180, v180\n"
    "v_exp_f32_e32 v181, v181\n"
    "v_exp_f32_e32 v182, v182\n"
    "v_exp_f32_e32 v183, v183\n"
    "s_waitcnt lgkmcnt(3)\n"
    "buffer_store_dwordx4 a[64:67], %[ost], s[52:55], s57 offen sc1\n"
    "v_exp_f32_e32 v184, v184\n"
    "v_exp_f32_e32 v185, v185\n"
    "v_exp_f32_e32 v186, v186\n"
    "v_exp_f32_e32 v187, v187\n"
    "v_exp_f32_e32 v188, v188\n"
    "v_exp_f32_e32 v189, v189\n"
    "v_exp_f32_e32 v190, v190\n"
    "v_exp_f32_e32 v191, v191\n"
    "s_waitcnt lgkmcnt(2)\n"
    "buffer_store_dwordx4 a[68:71], %[ost], s[52:55], s57 offen offset:1024 sc1\n"
    "v_cvt_pk_bf16_f32 v176, v176, v177\n"
    "v_cvt_pk_bf16_f32 v177, v178, v179\n"
    "v_cvt_pk_bf16_f32 v178, v180, v181\n"
    "v_cvt_pk_bf16_f32 v179, v182, v183\n"
    "v_cvt_pk_bf16_f32 v184, v184, v185\n"
    "v_cvt_pk_bf16_f32 v185, v186, v187\n"
    "v_mfma_f32_16x16x32_bf16 a[236:239], a[240:243], v[176:179], a[236:239]\n"
    "v_cvt_pk_bf16_f32 v186, v188, v189\n"
    "v_cvt_pk_bf16_f32 v187, v190, v191\n"
    "v_mfma_f32_16x16x32_bf16 v[96:99], a[128:131], v[176:179], v[96:99]\n"
    "v_mul_f32_e32 v221, s66, v216\n"
    "v_fmac_f32_e32 v221, s66, v217\n"
    "v_mfma_f32_16x16x32_bf16 a[236:239], a[240:243], v[184:187], a[236:239]\n"
    "s_waitcnt lgkmcnt(1)\n"
    "buffer_store_dwordx4 a[72:75], %[ost], s[52:55], s57 offen offset:2048 sc1\n"
    "v_fmac_f32_e32 v221, s66, v218\n"
    "v_mfma_f32_16x16x32_bf16 v[96:99], a[132:135], v[184:187], v[96:99]\n"
    "v_mfma_f32_16x16x32_bf16 v[100:103], a[136:139], v[176:179], v[100:103]\n"
    "s_nop 2\n"
    "v_accvgpr_read_b32 v219, a236\n"
    "v_rcp_f32_e32 v215, v219\n"
    "v_mfma_f32_16x16x32_bf16 v[100:103], a[140:143], v[184:187], v[100:103]\n"
    "s_nop 0\n"
    "v_mul_f32_e32 v96, v215, v96\n"
    "v_mul_f32_e32 v97, v215, v97\n"
    "v_mfma_f32_16x16x32_bf16 v[104:107], a[144:147], v[176:179], v[104:107]\n"
    "s_waitcnt lgkmcnt(0)\n"
    "buffer_store_dwordx4 a[76:79], %[ost], s[52:55], s57 offen offset:3072 sc1\n"
    "v_mul_f32_e32 v98, v215, v98\n"
    "v_mfma_f32_16x16x32_bf16 v[104:107], a[148:151], v[184:187], v[104:107]\n"
    "v_mul_f32_e32 v99, v215, v99\n"
    "v_mul_f32_e32 v100, v215, v100\n"
    "v_mfma_f32_16x16x32_bf16 v[108:111], a[152:155], v[176:179], v[108:111]\n"
    "v_mul_f32_e32 v101, v215, v101\n"
    "v_mul_f32_e32 v102, v215, v102\n"
    "v_mfma_f32_16x16x32_bf16 v[108:111], a[156:159], v[184:187], v[108:111]\n"
    "v_mul_f32_e32 v103, v215, v103\n"
    "v_cvt_pk_bf16_f32 v96, v96, v97\n"
    "v_mfma_f32_16x16x32_bf16 v[112:115], a[160:163], v[176:179], v[112:115]\n"
    "v_cvt_pk_bf16_f32 v97, v98, v99\n"
    "v_cvt_pk_bf16_f32 v98, v100, v101\n"
    "v_mfma_f32_16x16x32_bf16 v[112:115], a[164:167], v[184:187], v[112:115]\n"
    "ds_write_b64 %[ow0], v[96:97] offset:4096\n"
    "v_mfma_f32_16x16x32_bf16 v[116:119], a[168:171], v[176:179], v[116:119]\n"
    "v_cvt_pk_bf16_f32 v99, v102, v103\n"
    "v_mul_f32_e32 v104, v215, v104\n"
    "v_mfma_f32_16x16x32_bf16 v[116:119], a[172:175], v[184:187], v[116:119]\n"
    "ds_write_b64 %[ow1], v[98:99] offset:4096\n"
    "v_mfma_f32_16x16x32_bf16 v[120:123], a[176:179], v[176:179], v[120:123]\n"
    "v_mul_f32_e32 v105, v215, v105\n"
    "v_mul_f32_e32 v106, v215, v106\n"
    "v_mfma_f32_16x16x32_bf16 v[120:123], a[180:183], v[184:187], v[120:123]\n"
    "v_mul_f32_e32 v107, v215, v107\n"
    "v_mul_f32_e32 v108, v215, v108\n"
    "v_mfma_f32_16x16x32_bf16 v[124:127], a[184:187], v[176:179], v[124:127]\n"
    "v_mul_f32_e32 v109, v215, v109\n"
    "v_mul_f32_e32 v110, v215, v110\n"
    "v_mfma_f32_16x16x32_bf16 v[124:127], a[188:191], v[184:187], v[124:127]\n"
    "v_mul_f32_e32 v111, v215, v111\n"
    "v_cvt_pk_bf16_f32 v104, v104, v105\n"
    "v_mfma_f32_16x16x32_bf16 a[244:247], a[240:243], v[96:99], a[244:247]\n"
    "v_cvt_pk_bf16_f32 v105, v106, v107\n"
    "v_cvt_pk_bf16_f32 v106, v108, v109\n"
    "v_cvt_pk_bf16_f32 v107, v110, v111\n"
    "v_mul_f32_e32 v112, v215, v112\n"
    "v_mul_f32_e32 v113, v215, v113\n"
    "v_mfma_f32_16x16x32_bf16 a[244:247], a[240:243], v[104:107], a[244:247]\n"
    "ds_write_b64 %[ow2], v[104:105] offset:4096\n"
    "ds_write_b64 %[ow3], v[106:107] offset:4096\n"
    "v_mul_f32_e32 v114, v215, v114\n"
    "v_mul_f32_e32 v115, v215, v115\n"
    "v_mul_f32_e32 v116, v215, v116\n"
    "v_mul_f32_e32 v117, v215, v117\n"
    "v_mul_f32_e32 v118, v215, v118\n"
    "v_mul_f32_e32 v119, v215, v119\n"
    "v_cvt_pk_bf16_f32 v112, v112, v113\n"
    "v_cvt_pk_bf16_f32 v113, v114, v115\n"
    "v_cvt_pk_bf16_f32 v114, v116, v117\n"
    "v_cvt_pk_bf16_f32 v115, v118, v119\n"
    "v_mul_f32_e32 v120, v215, v120\n"
    "v_mul_f32_e32 v121, v215, v121\n"
    "v_mfma_f32_16x16x32_bf16 a[244:247], a[240:243], v[112:115], a[244:247]\n"
    "ds_write_b64 %[ow0], v[112:113] offset:4224\n"
    "ds_write_b64 %[ow1], v[114:115] offset:4224\n"
    "v_mul_f32_e32 v122, v215, v122\n"
    "v_mul_f32_e32 v123, v215, v123\n"
    "v_mul_f32_e32 v124, v215, v124\n"
    "v_mul_f32_e32 v125, v215, v125\n"
    "v_mul_f32_e32 v126, v215, v126\n"
    "v_mul_f32_e32 v127, v215, v127\n"
    "v_cvt_pk_bf16_f32 v120, v120, v121\n"
    "v_cvt_pk_bf16_f32 v121, v122, v123\n"
    "v_cvt_pk_bf16_f32 v122, v124, v125\n"
    "v_cvt_pk_bf16_f32 v123, v126, v127\n"
    "v_fmac_f32_e32 v221, s66, v219\n"
    "ds_write_b64 %[ow2], v[120:121] offset:4224\n"
    "v_mfma_f32_16x16x32_bf16 a[244:247], a[240:243], v[120:123], a[244:247]\n"
    "ds_write_b64 %[ow3], v[122:123] offset:4224\n"
    "ds_read_b128 a[80:83], %[or0] offset:4096\n"
    "ds_read_b128 a[84:87], %[or1] offset:5120\n"
    "ds_read_b128 a[88:91], %[or0] offset:6144\n"
    "ds_read_b128 a[92:95], %[or1] offset:7168\n"
    "s_nop 2\n"
    "v_accvgpr_read_b32 v220, a244\n"
    "v_add_f32_e32 v221, v221, v220\n"
    "v_cmp_nlt_f32_e64 vcc, abs(v221), s67\n"
    "s_nop 1\n"
    "s_cmp_lg_u64 vcc, 0\n"
    "s_cselect_b32 s70, s64, 0\n"
    "s_or_b32 s63, s63, s70\n"
    "s_waitcnt lgkmcnt(3)\n"
    "buffer_store_dwordx4 a[80:83], %[ost], s[52:55], s58 offen sc1\n"
    "s_waitcnt lgkmcnt(2)\n"
    "buffer_store_dwordx4 a[84:87], %[ost], s[52:55], s58 offen offset:1024 sc1\n"
    "s_waitcnt lgkmcnt(1)\n"
    "buffer_store_dwordx4 a[88:91], %[ost], s[52:55], s58 offen offset:2048 sc1\n"
    "s_waitcnt lgkmcnt(0)\n"
    "buffer_store_dwordx4 a[92:95], %[ost], s[52:55], s58 offen offset:3072 sc1\n"
    "s_mov_b32 %[flag], s63\n"
    ".Lattn_end%=:\n"
    : [flag] "=s"(flag)
    : [q] "s"(Q), [k] "s"(K), [v] "s"(V), [o] "s"(O), [w] "s"(w), [wg] "s"(blockIdx.x), [wo] "s"((unsigned)wg << 18), [wo2] "s"((unsigned)wg << 19), [pf] "v"(pf),
      [kb] "v"(kb), [qb] "v"(kb + stage), [vb0] "v"(vbb + 16u * ((0u + (p4 >> 1)) ^ xg)), [vb1] "v"(vbb + 16u * ((2u + (p4 >> 1)) ^ xg)),
      [vo0] "v"(vo + 16u * (p4 ^ 0u)), [vo1] "v"(vo + 16u * (p4 ^ 1u)), [vo2] "v"(vo + 16u * (p4 ^ 2u)), [vo3] "v"(vo + 16u * (p4 ^ 3u)),
      [ow0] "v"(owb + 16u * ((0u + (g4 >> 1)) ^ (c16 & 7u))), [ow1] "v"(owb + 16u * ((2u + (g4 >> 1)) ^ (c16 & 7u))),
      [ow2] "v"(owb + 16u * ((4u + (g4 >> 1)) ^ (c16 & 7u))), [ow3] "v"(owb + 16u * ((6u + (g4 >> 1)) ^ (c16 & 7u))),
      [or0] "v"(orb + 16u * (c16 ^ g4)), [or1] "v"(orb + 16u * (c16 ^ (4u + g4))),
      [ost] "v"(16u * lane), [vq] "v"(256u * c16 + 16u * g4), [ones] "v"(ones)
    : "v0", "v1", "v2", "v3", "v4", "v5", "v6", "v7", "v8", "v9", "v10", "v11", "v12", "v13", "v14", "v15", "v16", "v17", "v18", "v19", "v20", "v21", "v22", "v23", "v24", "v25", "v26", "v27", "v28", "v29", "v30", "v31", "v32", "v33", "v34", "v35", "v36", "v37", "v38", "v39", "v40", "v41", "v42", "v43", "v44", "v45", "v46", "v47", "v48", "v49", "v50", "v51", "v52", "v53", "v54", "v55", "v56", "v57", "v58", "v59", "v60", "v61", "v62", "v63", "v64", "v65", "v66", "v67", "v68", "v69", "v70", "v71", "v72", "v73", "v74", "v75", "v76", "v77", "v78", "v79", "v80", "v81", "v82", "v83", "v84", "v85", "v86", "v87", "v88", "v89", "v90", "v91", "v92", "v93", "v94", "v95", "v96", "v97", "v98", "v99", "v100", "v101", "v102", "v103", "v104", "v105", "v106", "v107", "v108", "v109", "v110", "v111", "v112", "v113", "v114", "v115", "v116", "v117", "v118", "v119", "v120", "v121", "v122", "v123", "v124", "v125", "v126", "v127", "v128", "v129", "v130", "v131", "v132", "v133", "v134", "v135", "v136", "v137", "v138", "v139", "v140", "v141", "v142", "v143", "v144", "v145", "v146", "v147", "v148", "v149", "v150", "v151", "v152", "v153", "v154", "v155", "v156", "v157", "v158", "v159", "v160", "v161", "v162", "v163", "v164", "v165", "v166", "v167", "v168", "v169", "v170", "v171", "v172", "v173", "v174", "v175", "v176", "v177", "v178", "v179", "v180", "v181", "v182", "v183", "v184", "v185", "v186", "v187", "v188", "v189", "v190", "v191", "v192", "v193", "v194", "v195", "v196", "v197", "v198", "v199", "v200", "v201", "v202", "v203", "v204", "v205", "v206", "v207", "v208", "v209", "v210", "v211", "v212", "v213", "v214", "v215", "v216", "v217", "v218", "v219", "v220", "v221", "v222", "v223", "a0", "a1", "a2", "a3", "a4", "a5", "a6", "a7", "a8", "a9", "a10", "a11", "a12", "a13", "a14", "a15", "a16", "a17", "a18", "a19", "a20", "a21", "a22", "a23", "a24", "a25", "a26", "a27", "a28", "a29", "a30", "a31", "a32", "a33", "a34", "a35", "a36", "a37", "a38", "a39", "a40", "a41", "a42", "a43", "a44", "a45", "a46", "a47", "a48", "a49", "a50", "a51", "a52", "a53", "a54", "a55", "a56", "a57", "a58", "a59", "a60", "a61", "a62", "a63", "a64", "a65", "a66", "a67", "a68", "a69", "a70", "a71", "a72", "a73", "a74", "a75", "a76", "a77", "a78", "a79", "a80", "a81", "a82", "a83", "a84", "a85", "a86", "a87", "a88", "a89", "a90", "a91", "a92", "a93", "a94", "a95", "a96", "a97", "a98", "a99", "a100", "a101", "a102", "a103", "a104", "a105", "a106", "a107", "a108", "a109", "a110", "a111", "a112", "a113", "a114", "a115", "a116", "a117", "a118", "a119", "a120", "a121", "a122", "a123", "a124", "a125", "a126", "a127", "a128", "a129", "a130", "a131", "a132", "a133", "a134", "a135", "a136", "a137", "a138", "a139", "a140", "a141", "a142", "a143", "a144", "a145", "a146", "a147", "a148", "a149", "a150", "a151", "a152", "a153", "a154", "a155", "a156", "a157", "a158", "a159", "a160", "a161", "a162", "a163", "a164", "a165", "a166", "a167", "a168", "a169", "a170", "a171", "a172", "a173", "a174", "a175", "a176", "a177", "a178", "a179", "a180", "a181", "a182", "a183", "a184", "a185", "a186", "a187", "a188", "a189", "a190", "a191", "a192", "a193", "a194", "a195", "a196", "a197", "a198", "a199", "a200", "a201", "a202", "a203", "a204", "a205", "a206", "a207", "a208", "a209", "a210", "a211", "a212", "a213", "a214", "a215", "a216", "a217", "a218", "a219", "a220", "a221", "a222", "a223", "a224", "a225", "a226", "a227", "a228", "a229", "a230", "a231", "a232", "a233", "a234", "a235", "a236", "a237", "a238", "a239", "a240", "a241", "a242", "a243", "a244", "a245", "a246", "a247", "a248", "a249", "a250", "a251", "a252", "a253", "a254", "a255", "s36", "s37", "s38", "s39", "s40", "s41", "s42", "s43", "s44", "s45", "s46", "s47", "s48", "s49", "s50", "s51", "s52", "s53", "s54", "s55", "s56", "s57", "s58", "s59", "s60", "s61", "s62", "s63", "s64", "s65", "s66", "s67", "s68", "s69", "s70", "s71", "s72", "s73", "s74", "s75", "s76", "s77", "s78", "s79", "s84", "s85", "s94", "vcc", "scc", "memory");
  flag >>= 1;
  if (flag) {
    asm volatile("s_waitcnt vmcnt(0)" ::: "memory");
    const char* q = Q + wg * 0x40000; const char* k = K + wg * 0x80000; const char* v = V + wg * 0x80000; char* o = O + wg * 0x40000;
    for (int i = 0; i < 4; ++i)
      if (flag & (1u << i)) {
        const unsigned row = 64u * w + lane;
        exact_rows((const u32x4*)(q + i * 0x10000 + row * 256u), (const u32x4*)(k + i * 0x20000), (const u32x4*)(v + i * 0x20000), (u32x4*)(o + i * 0x10000 + row * 256u));
      }
  }
}

extern "C" void kernel_launch(void* const* d_in, const int* in_sizes, int n_in, void* d_out, int out_size, void* d_ws, size_t ws_size, hipStream_t stream) {
  (void)hipFuncSetAttribute((const void*)attn_fwd_kernel, hipFuncAttributeMaxDynamicSharedMemorySize, 163840);
  attn_fwd_kernel<<<256, 256, 163840, stream>>>((const char*)d_in[0], (const char*)d_in[1], (const char*)d_in[2], (char*)d_out);
}
```
